```python
import torch

def _softmax(x, axis=-1):
    return torch.softmax(x, dim=axis)

def _gelu(x):
    return torch.nn.functional.gelu(x, approximate='tanh')

def _silu(x):
    return torch.nn.functional.silu(x)

def _sigmoid(x):
    return torch.sigmoid(x)

def _layernorm(x, axis=-1, eps=1e-05):
    m = x.mean(dim=axis, keepdim=True)
    v = x.var(dim=axis, keepdim=True, unbiased=False)
    return (x - m) / torch.sqrt(v + eps)

def _scatter_add(tgt, idx, src, axis=0):
    return tgt.clone().index_add_(axis, idx, src)

def _segment_sum(src, seg, n):
    out = torch.zeros((n,) + src.shape[1:], dtype=src.dtype)
    return out.index_add_(0, seg, src)

def _pad(x, pw):
    flat = tuple((v for pair in reversed(pw) for v in pair))
    return torch.nn.functional.pad(x, flat)
kernel_name = 'adaLN_cond__linear_kernel_hedgehog__glu_gated_bilinear__prompt'

def setup_inputs():

    _w = lambda a: (lambda t: t.to(torch.bfloat16) if t.is_floating_point() else t)(torch.as_tensor(a))

    def _A_setup():
        B, S, D, H = (1, 2048, 448, 16)
        Dh = D // H
        return {'x': _w((lambda *s: torch.randn(*s, dtype=torch.bfloat16) * 0.05)(B, S, D)), 'wqkv': _w((lambda *s: torch.randn(*s, dtype=torch.bfloat16) * 0.4)(D, 3 * D)), 'w_phi': _w((lambda *s: torch.randn(*s, dtype=torch.bfloat16) * 0.4)(H, Dh, Dh) * Dh ** (-0.5)), 'wo': _w((lambda *s: torch.randn(*s, dtype=torch.bfloat16) * 0.05)(D, D))}
    _A_in = _A_setup()

    def _B_setup():
        B, S, D, F = (1, 2048, 448, 1792)
        return {'x': _w((lambda *s: torch.randn(*s, dtype=torch.bfloat16) * 0.05)(B, S, D)), 'w_gate': _w((lambda *s: torch.randn(*s, dtype=torch.bfloat16) * 1.6)(D, F) * D ** (-0.5)), 'w_up': _w((lambda *s: torch.randn(*s, dtype=torch.bfloat16) * 1.6)(D, F) * D ** (-0.5)), 'w_down': _w((lambda *s: torch.randn(*s, dtype=torch.bfloat16) * 1.6)(F, D) * F ** (-0.5))}
    _B_in = _B_setup()
    return {'A_x': _A_in['x'], 'A_wqkv': _A_in['wqkv'], 'A_w_phi': _A_in['w_phi'], 'A_wo': _A_in['wo'], 'B_x': _B_in['x'], 'B_w_gate': _B_in['w_gate'], 'B_w_up': _B_in['w_up'], 'B_w_down': _B_in['w_down'], 'c': _w((lambda *s: torch.randn(*s, dtype=torch.bfloat16) * 0.1)(1, 448) * 8.0), 'w_mod': _w((lambda *s: torch.randn(*s, dtype=torch.bfloat16) * 0.1)(448, 6 * 448) * 448 ** (-0.5) * 8.0)}

def reference(A_x, A_wqkv, A_w_phi, A_wo, B_x, B_w_gate, B_w_up, B_w_down, c, w_mod):

    def _A_ref(x, wqkv, w_phi, wo):
        B, S, D = x.shape
        H = 16
        Dh = D // H
        qkv = (lambda x, *d: x.permute(*d))((x @ wqkv).reshape(B, S, 3, H, Dh), 2, 0, 3, 1, 4)
        q, k, v = (qkv[0], qkv[1], qkv[2])
        pq = torch.einsum('bhsd,hdf->bhsf', q, w_phi)
        pk = torch.einsum('bhsd,hdf->bhsf', k, w_phi)
        phi_q = torch.concatenate([_softmax(pq, axis=-1), _softmax(-pq, axis=-1)], axis=-1)
        phi_k = torch.concatenate([_softmax(pk, axis=-1), _softmax(-pk, axis=-1)], axis=-1)
        kv = torch.einsum('bhsf,bhsd->bhfd', phi_k, v)
        z = torch.einsum('bhsf->bhf', phi_k)
        num = torch.einsum('bhqf,bhfd->bhqd', phi_q, kv)
        den = torch.einsum('bhqf,bhf->bhq', phi_q, z)[..., None] + 1e-06
        o = num / den
        return (lambda x, *d: x.permute(*d))(o, 0, 2, 1, 3).reshape(B, S, D) @ wo

    def _B_ref(x, w_gate, w_up, w_down):
        g = torch.einsum('bsd,df->bsf', x, w_gate)
        u = torch.einsum('bsd,df->bsf', x, w_up)
        h = g * u
        return torch.einsum('bsf,fd->bsd', h, w_down)
    x = A_x
    mod = (_silu(c) @ w_mod).reshape(c.shape[0], 6, 1, -1)
    sa, ca, ga, sb, cb, gb = (mod[:, 0].clone(), mod[:, 1].clone(), mod[:, 2].clone(), mod[:, 3].clone(), mod[:, 4].clone(), mod[:, 5].clone())
    h = _layernorm(x, axis=-1) * (1 + ca) + sa
    x = x + ga * _A_ref(h, A_wqkv, A_w_phi, A_wo)
    h = _layernorm(x, axis=-1) * (1 + cb) + sb
    x = x + gb * _B_ref(h, B_w_gate, B_w_up, B_w_down)
    return x
```

```cpp
#include <hip/hip_runtime.h>
#include <hip/hip_bf16.h>
#include <stdint.h>

typedef unsigned short u16;
static __device__ __forceinline__ float bf2f(u16 b){ return __uint_as_float(((unsigned)b)<<16); }
static __device__ __forceinline__ u16 f2bf(float f){ __hip_bfloat16 h = __float2bfloat16(f); return *(u16*)&h; }
static __device__ __forceinline__ float rbf(float f){ return bf2f(f2bf(f)); }

constexpr int S=2048, D=448, H=16, DH=28, F=1792;

__global__ void k_mod(const u16* c, const u16* wmod, float* mod){
  int n = blockIdx.x*64 + (threadIdx.x&63);
  int part = threadIdx.x>>6;
  __shared__ float red[4][64];
  float acc=0;
  for(int k=part*112;k<part*112+112;k++){
    float cv=bf2f(c[k]); float s = rbf(cv/(1.f+__expf(-cv)));
    acc += s*bf2f(wmod[(size_t)k*(6*D)+n]);
  }
  red[part][threadIdx.x&63]=acc; __syncthreads();
  if(part==0){ int l=threadIdx.x; mod[n]=rbf(red[0][l]+red[1][l]+red[2][l]+red[3][l]); }
}
template<bool XF32>
__global__ void k_ln(const void* xin, const float* sh, const float* cs, u16* h){
  int row=blockIdx.x; int t=threadIdx.x;
  float v[7]; float s=0;
  for(int i=0;i<7;i++){ int k=t+64*i; v[i]= XF32? ((const float*)xin)[(size_t)row*D+k] : bf2f(((const u16*)xin)[(size_t)row*D+k]); s+=v[i]; }
  for(int o=32;o;o>>=1) s+=__shfl_xor(s,o);
  float m=s/D; float q=0;
  for(int i=0;i<7;i++){ float d=v[i]-m; q+=d*d; }
  for(int o=32;o;o>>=1) q+=__shfl_xor(q,o);
  float r=rsqrtf(q/D+1e-5f);
  for(int i=0;i<7;i++){ int k=t+64*i; h[(size_t)row*D+k]=f2bf((v[i]-m)*r*(1.f+cs[k])+sh[k]); }
}
__global__ void k_gemm(const u16* A, const u16* B, float* C, int M, int N, int K){
  __shared__ float As[16][17], Bs[16][17];
  int tx=threadIdx.x&15, ty=threadIdx.x>>4;
  int row=blockIdx.y*16+ty, col=blockIdx.x*16+tx;
  float acc=0;
  for(int k0=0;k0<K;k0+=16){
    As[ty][tx]=bf2f(A[(size_t)row*K+k0+tx]);
    Bs[ty][tx]=bf2f(B[(size_t)(k0+ty)*N+col]);
    __syncthreads();
    for(int k=0;k<16;k++) acc+=As[ty][k]*Bs[k][tx];
    __syncthreads();
  }
  C[(size_t)row*N+col]=acc;
}
__global__ void k_phi(const float* qkv, const u16* wphi, float* phiq, float* phik){
  int idx=blockIdx.x*blockDim.x+threadIdx.x;
  int which=idx&1; int h=(idx>>1)&15; int s=idx>>5;
  const float* src=qkv+(size_t)s*3*D+which*D+h*DH;
  float q[DH]; for(int d=0;d<DH;d++) q[d]=rbf(src[d]);
  float p[DH]; float mx=-1e30f, mn=1e30f;
  for(int f=0;f<DH;f++){ float a=0; for(int d=0;d<DH;d++) a+=q[d]*bf2f(wphi[(h*DH+d)*DH+f]); a=rbf(a); p[f]=a; mx=fmaxf(mx,a); mn=fminf(mn,a);}
  float s1=0,s2=0; float e1[DH], e2[DH];
  for(int f=0;f<DH;f++){ e1[f]=__expf(p[f]-mx); e2[f]=__expf(mn-p[f]); s1+=e1[f]; s2+=e2[f]; }
  float* dst=(which?phik:phiq)+((size_t)s*H+h)*56;
  for(int f=0;f<DH;f++){ dst[f]=rbf(e1[f]/s1); dst[DH+f]=rbf(e2[f]/s2); }
}
__global__ void k_kv(const float* phik, const float* qkv, float* kv){
  int h=blockIdx.x; int f=blockIdx.y; int d=threadIdx.x&31; int part=threadIdx.x>>5;
  __shared__ float red[8][32];
  float acc=0;
  for(int s=part;s<S;s+=8){ float pk=phik[((size_t)s*H+h)*56+f]; float v = d<DH? rbf(qkv[(size_t)s*3*D+2*D+h*DH+d]) : 1.f; acc+=pk*v; }
  red[part][d]=acc; __syncthreads();
  if(part==0 && d<=DH){ float a=0; for(int i=0;i<8;i++) a+=red[i][d]; kv[(h*56+f)*29+d]=rbf(a); }
}
__global__ void k_o(const float* phiq, const float* kv, u16* o){
  int idx=blockIdx.x*blockDim.x+threadIdx.x;
  int d=idx&31; int h=(idx>>5)&15; int s=idx>>9;
  if(d>=DH) return;
  float num=0, den=0;
  for(int f=0;f<56;f++){ float p=phiq[((size_t)s*H+h)*56+f]; num+=p*kv[(h*56+f)*29+d]; den+=p*kv[(h*56+f)*29+28]; }
  o[(size_t)s*D+h*DH+d]=f2bf(rbf(num)/(rbf(den)+1e-6f));
}
__global__ void k_res1(const u16* x, const float* attn, const float* ga, float* x1){
  int i=blockIdx.x*blockDim.x+threadIdx.x; int k=i%D;
  x1[i]=rbf(bf2f(x[i])+rbf(ga[k]*rbf(attn[i])));
}
__global__ void k_act(const float* g, const float* u, u16* a){
  int i=blockIdx.x*blockDim.x+threadIdx.x; a[i]=f2bf(rbf(g[i])*rbf(u[i]));
}
__global__ void k_res2(const float* x1, const float* dn, const float* gb, u16* out){
  int i=blockIdx.x*blockDim.x+threadIdx.x; int k=i%D;
  out[i]=f2bf(x1[i]+rbf(gb[k]*rbf(dn[i])));
}

extern "C" void kernel_launch(void* const* d_in, const int* in_sizes, int n_in,
                              void* d_out, int out_size, void* d_ws, size_t ws_size,
                              hipStream_t stream) {
  const u16* x=(const u16*)d_in[0]; const u16* wqkv=(const u16*)d_in[1]; const u16* wphi=(const u16*)d_in[2]; const u16* wo=(const u16*)d_in[3];
  const u16* wg=(const u16*)d_in[5]; const u16* wu=(const u16*)d_in[6]; const u16* wd=(const u16*)d_in[7];
  const u16* c=(const u16*)d_in[8]; const u16* wmod=(const u16*)d_in[9];
  char* ws=(char*)d_ws; size_t off=0;
  auto alloc=[&](size_t b){ char* p=ws+off; off+=(b+255)&~(size_t)255; return p; };
  float* mod=(float*)alloc(6*D*4);
  u16* h=(u16*)alloc((size_t)S*D*2);
  float* qkv=(float*)alloc((size_t)S*3*D*4);
  float* phiq=(float*)alloc((size_t)S*H*56*4);
  float* phik=(float*)alloc((size_t)S*H*56*4);
  float* kv=(float*)alloc(H*56*29*4);
  u16* o=(u16*)alloc((size_t)S*D*2);
  float* attn=(float*)alloc((size_t)S*D*4);
  float* x1=(float*)alloc((size_t)S*D*4);
  u16* h2=(u16*)alloc((size_t)S*D*2);
  float* g=(float*)alloc((size_t)S*F*4);
  float* u=(float*)alloc((size_t)S*F*4);
  u16* act=(u16*)alloc((size_t)S*F*2);
  float* dn=(float*)alloc((size_t)S*D*4);
  k_mod<<<6*D/64,256,0,stream>>>(c,wmod,mod);
  k_ln<false><<<S,64,0,stream>>>(x,mod,mod+D,h);
  k_gemm<<<dim3(3*D/16,S/16),256,0,stream>>>(h,wqkv,qkv,S,3*D,D);
  k_phi<<<S*H*2/256,256,0,stream>>>(qkv,wphi,phiq,phik);
  k_kv<<<dim3(H,56),256,0,stream>>>(phik,qkv,kv);
  k_o<<<S*H*32/256,256,0,stream>>>(phiq,kv,o);
  k_gemm<<<dim3(D/16,S/16),256,0,stream>>>(o,wo,attn,S,D,D);
  k_res1<<<S*D/256,256,0,stream>>>(x,attn,mod+2*D,x1);
  k_ln<true><<<S,64,0,stream>>>(x1,mod+3*D,mod+4*D,h2);
  k_gemm<<<dim3(F/16,S/16),256,0,stream>>>(h2,wg,g,S,F,D);
  k_gemm<<<dim3(F/16,S/16),256,0,stream>>>(h2,wu,u,S,F,D);
  k_act<<<S*F/256,256,0,stream>>>(g,u,act);
  k_gemm<<<dim3(D/16,S/16),256,0,stream>>>(act,wd,dn,S,D,F);
  k_res2<<<S*D/256,256,0,stream>>>(x1,dn,mod+5*D,(u16*)d_out);
}
```

```cpp
#include <hip/hip_runtime.h>
#include <stdint.h>

typedef unsigned u32;
typedef unsigned long long u64;
typedef unsigned short u16;
typedef u32 v4u __attribute__((ext_vector_type(4)));
typedef u32 v2u __attribute__((ext_vector_type(2)));
typedef float f32x2 __attribute__((ext_vector_type(2)));
typedef float f32x4 __attribute__((ext_vector_type(4)));
typedef float f32x16 __attribute__((ext_vector_type(16)));
typedef __bf16 bf16x2 __attribute__((ext_vector_type(2)));
typedef __bf16 bf16x8 __attribute__((ext_vector_type(8)));
typedef short s16x4 __attribute__((ext_vector_type(4)));
typedef __amdgpu_buffer_rsrc_t rsrc_t;
#define LAS __attribute__((address_space(3)))
#define DI __device__ __forceinline__

constexpr u32 WS_EPOCH = 0;
constexpr u32 WS_TMO = 256;
constexpr u32 WS_FLAG = 4096;
constexpr u32 FL_KV = 0, FL_O = 256, FL_X = 512, FL_C = 768, FL_A = 1024;
constexpr u32 WS_MODG = 0x80000;
constexpr u32 MODG_REP = 2688 * 8;
constexpr u32 WS_CSB = 0xB0000;
constexpr u32 WS_STAT = 0xC0000;
constexpr u32 WS_KVP = 0x100000;
constexpr u32 WS_OFR = 0x200000;
constexpr u32 WS_X1F = 0x400000;
constexpr u32 WS_ACT = 0x600000;
constexpr u32 WS_DBG = 0xE00000;
constexpr u32 WS_USED = 0x1000000;

constexpr u32 L_XS = 0;
constexpr u32 L_WKV = 0, L_WQ = 57344;
constexpr u32 L_PHI = 86016, L_VFR = 102400, L_KVR = 110592;
constexpr u32 L_T1 = 114688;
constexpr u32 L_ST1 = 118272;
constexpr u32 L_WPH = 119296;
constexpr u32 L_P0S = 121344;
constexpr u32 L_CSP = 123392;
constexpr u32 L_CSQ = 131584;
constexpr u32 L_CSF = 135680;
constexpr u32 L_T2 = 136448;
constexpr u32 L_WO = 0, L_XCH = 57344;
constexpr u32 L_AG = 0, L_AU = 57344;
constexpr u32 L_B1G = 114688, L_B1U = 136320;
constexpr u32 L_B2G = 0, L_B2U = 57472;
constexpr u32 L_TGA = 157824, L_TG2 = 158080, L_TGB = 158336, L_TCS = 158592;
constexpr u32 L_WD0 = 0, L_WD1 = 73728;
constexpr u32 LDS_BYTES = 163840;

constexpr int SPIN_CAP = 40000;

DI rsrc_t mk(const void* p, u32 bytes) { return __builtin_amdgcn_make_buffer_rsrc((void*)p, 0, (int)bytes, 0x00020000); }
DI float bflo(u32 v) { return __uint_as_float(v << 16); }
DI float bfhi(u32 v) { return __uint_as_float(v & 0xffff0000u); }
DI u32 pk2(float a, float b) { f32x2 v = {a, b}; return __builtin_bit_cast(u32, __builtin_convertvector(v, bf16x2)); }
DI float rbf(float a) { return bflo(pk2(a, a)); }
DI float dot2(u32 a, u32 b, float c) { return __builtin_amdgcn_fdot2_f32_bf16(__builtin_bit_cast(bf16x2, a), __builtin_bit_cast(bf16x2, b), c, false); }
DI bf16x8 asb(v4u v) { return __builtin_bit_cast(bf16x8, v); }
DI f32x16 mfma(v4u a, v4u b, f32x16 c) { return __builtin_amdgcn_mfma_f32_32x32x16_bf16(asb(a), asb(b), c, 0, 0, 0); }
DI float half_lo(float v) { auto s = __builtin_amdgcn_permlane32_swap(__float_as_uint(v), __float_as_uint(v), false, false); return __uint_as_float(s[0]); }
DI float half_sum(float v) { auto s = __builtin_amdgcn_permlane32_swap(__float_as_uint(v), __float_as_uint(v), false, false); return __uint_as_float(s[0]) + __uint_as_float(s[1]); }
DI float half_max(float v) { auto s = __builtin_amdgcn_permlane32_swap(__float_as_uint(v), __float_as_uint(v), false, false); return fmaxf(__uint_as_float(s[0]), __uint_as_float(s[1])); }
DI float half_min(float v) { auto s = __builtin_amdgcn_permlane32_swap(__float_as_uint(v), __float_as_uint(v), false, false); return fminf(__uint_as_float(s[0]), __uint_as_float(s[1])); }
DI float half_hi(float v) { auto s = __builtin_amdgcn_permlane32_swap(__float_as_uint(v), __float_as_uint(v), false, false); return __uint_as_float(s[1]); }
template <int CTRL> DI float dpp_add(float v) { return v + __int_as_float(__builtin_amdgcn_update_dpp(0, __float_as_int(v), CTRL, 0xf, 0xf, true)); }
#define ROR2 0x122
#define ROR4 0x124
#define ROR8 0x128
#define BAR() asm volatile("s_waitcnt lgkmcnt(0)\n\ts_barrier" ::: "memory")
#define VMW(n) asm volatile("s_waitcnt vmcnt(" #n ")" ::: "memory")
#define SCHED() __builtin_amdgcn_sched_barrier(0)

DI void dma16(u32 voff, rsrc_t rs, u32 soff, u32 ldsaddr) {
  asm volatile("s_mov_b32 m0, %3\n\ts_nop 0\n\tbuffer_load_dwordx4 %0, %1, %2 offen lds" ::"v"(voff), "s"(rs), "s"(soff), "s"(ldsaddr) : "memory");
}
DI v4u ld128(rsrc_t r, u32 off) { return __builtin_amdgcn_raw_buffer_load_b128(r, off, 0, 0); }
DI v4u ld128c(rsrc_t r, u32 off) { return __builtin_amdgcn_raw_buffer_load_b128(r, off, 0, 16); }
DI v2u ld64(rsrc_t r, u32 off) { return __builtin_amdgcn_raw_buffer_load_b64(r, off, 0, 0); }
DI v2u ld64c(rsrc_t r, u32 off) { return __builtin_amdgcn_raw_buffer_load_b64(r, off, 0, 16); }
DI void st128c(rsrc_t r, u32 off, v4u v) { __builtin_amdgcn_raw_buffer_store_b128(v, r, off, 0, 16); }
DI void st64c(rsrc_t r, u32 off, v2u v) { __builtin_amdgcn_raw_buffer_store_b64(v, r, off, 0, 16); }
DI void st32c(rsrc_t r, u32 off, u32 v) { __builtin_amdgcn_raw_buffer_store_b32(v, r, off, 0, 16); }

struct Lds {
  LAS unsigned char* p;
  DI v4u r128(u32 o) const { return *(LAS v4u*)(p + o); }
  DI v2u r64(u32 o) const { return *(LAS v2u*)(p + o); }
  DI f32x4 rf4(u32 o) const { return *(LAS f32x4*)(p + o); }
  DI f32x2 rf2(u32 o) const { return *(LAS f32x2*)(p + o); }
  DI float rf(u32 o) const { return *(LAS float*)(p + o); }
  DI void w128(u32 o, v4u v) const { *(LAS v4u*)(p + o) = v; }
  DI void w64(u32 o, v2u v) const { *(LAS v2u*)(p + o) = v; }
  DI void wf4(u32 o, f32x4 v) const { *(LAS f32x4*)(p + o) = v; }
  DI void wf2(u32 o, f32x2 v) const { *(LAS f32x2*)(p + o) = v; }
  DI void wf(u32 o, float v) const { *(LAS float*)(p + o) = v; }
  DI v2u tr(u32 o) const { return __builtin_bit_cast(v2u, __builtin_amdgcn_ds_read_tr16_b64_v4i16((LAS s16x4*)(p + o))); }
  DI v4u tr2(u32 o0, u32 o1) const { v2u a = tr(o0), b = tr(o1); v4u r = {a.x, a.y, b.x, b.y}; return r; }
};

DI void wait_flags(rsrc_t ws, u32 off, bool active, u32 tlo, u32 thi, u32 code) {
  for (int spins = 0;; ++spins) {
    asm volatile("" ::: "memory");
    v2u v = ld64c(ws, off);
    bool ok = !active || (v.x == tlo && v.y == thi);
    if (__all(ok)) break;
    if (spins > SPIN_CAP) { st32c(ws, WS_TMO, code); break; }
    __builtin_amdgcn_s_sleep(1);
  }
  asm volatile("" ::: "memory");
}
DI void set_flag(rsrc_t ws, u32 idx, u32 tlo, u32 thi) { v2u t = {tlo, thi}; st64c(ws, WS_FLAG + idx * 256, t); }

#define STAMP(i)

DI void feature_map(const f32x16& pin, int h, u32 (&P1)[8], u32 (&P2)[8]) {
  const float L2E = 1.4426950408889634f, INF = __builtin_inff();
  float pm[16], pn[16];
#pragma unroll
  for (int r = 0; r < 16; ++r) { pm[r] = pin[r]; pn[r] = pin[r]; }
#pragma unroll
  for (int r = 12; r < 16; ++r) { pm[r] = h ? -INF : pin[r]; pn[r] = h ? INF : pin[r]; }
  float mx = pm[0], mn = pn[0];
#pragma unroll
  for (int r = 1; r < 16; ++r) { mx = fmaxf(mx, pm[r]); mn = fminf(mn, pn[r]); }
  mx = half_max(mx); mn = half_min(mn);
  const float cmx = -mx * L2E, cmn = mn * L2E;
  float e1[16], e2[16], s1 = 0.f, s2 = 0.f;
#pragma unroll
  for (int r = 0; r < 16; ++r) {
    e1[r] = __builtin_amdgcn_exp2f(fmaf(pm[r], L2E, cmx));
    e2[r] = __builtin_amdgcn_exp2f(fmaf(pn[r], -L2E, cmn));
    s1 += e1[r]; s2 += e2[r];
  }
  s1 = half_sum(s1); s2 = half_sum(s2);
  const float r1 = __builtin_amdgcn_rcpf(s1), r2 = __builtin_amdgcn_rcpf(s2);
#pragma unroll
  for (int i = 0; i < 8; ++i) { P1[i] = pk2(e1[2 * i] * r1, e1[2 * i + 1] * r1); P2[i] = pk2(e2[2 * i] * r2, e2[2 * i + 1] * r2); }
}

template <int NV> DI void gemv_reduce_store(const Lds& L, float (&acc)[NV][8], int w, int lane) {
#pragma unroll
  for (int v = 0; v < NV; ++v)
#pragma unroll
    for (int j = 0; j < 8; ++j) { float a = acc[v][j]; a = dpp_add<ROR2>(a); a = dpp_add<ROR4>(a); a = dpp_add<ROR8>(a); acc[v][j] = a; }
  if ((lane & 14) == 0) {
    const u32 base = L_P0S + (w * 4 + (lane >> 4)) * (NV * 64) + (lane & 1) * 32;
#pragma unroll
    for (int v = 0; v < NV; ++v) {
      f32x4 a = {acc[v][0], acc[v][1], acc[v][2], acc[v][3]}, b = {acc[v][4], acc[v][5], acc[v][6], acc[v][7]};
      L.wf4(base + v * 64, a); L.wf4(base + v * 64 + 16, b);
    }
  }
}

__global__ void __launch_bounds__(256, 1)
fused_block(const u16* __restrict__ gx, const u16* __restrict__ gwqkv, const u16* __restrict__ gwphi, const u16* __restrict__ gwo,
            const u16* __restrict__ gwg, const u16* __restrict__ gwu, const u16* __restrict__ gwd, const u16* __restrict__ gc,
            const u16* __restrict__ gwmod, u16* __restrict__ gout, unsigned char* __restrict__ gws) {
  extern __shared__ __attribute__((aligned(16))) unsigned char smem[];
  Lds L; L.p = (LAS unsigned char*)smem;
  const u32 lds0 = (u32)(uintptr_t)L.p;
  const int tid = threadIdx.x, lane = tid & 63;
  const int w = __builtin_amdgcn_readfirstlane(tid >> 6);
  const int bid = blockIdx.x, xq = bid & 7, iq = bid >> 3;
  const int h = lane >> 5, t32 = lane & 31, g16 = (lane >> 4) & 1, q4 = (lane & 15) >> 2, p4 = lane & 3;

  const rsrc_t rWS = mk(gws, WS_USED);
  const rsrc_t rX = mk(gx, 2048 * 896), rWQKV = mk(gwqkv, 448 * 2688), rWPHI = mk(gwphi, 16 * 784 * 2), rWO = mk(gwo, 448 * 896);
  const rsrc_t rWG = mk(gwg, 448 * 3584), rWU = mk(gwu, 448 * 3584), rWD = mk(gwd, 1792 * 896), rC = mk(gc, 896), rWM = mk(gwmod, 448 * 5376);
  const rsrc_t rOUT = mk(gout, 2048 * 896);

  const int hd = 8 * (xq & 1) + (iq & 7), tt = 4 * (xq >> 1) + (iq >> 3);
  const bool p24 = iq < 28;
  const int t64 = 4 * xq + iq / 7, cs = iq % 7;
  const int chunk = (iq >> 2) * 32 + xq * 4 + (iq & 3);
  const bool p0 = chunk < 168 && iq < 24, p0c = iq < 28;

  STAMP(0);
  const v2u ep = ld64c(rWS, WS_EPOCH);
  v4u wm[4]; u32 cv[4];
  if (p0) {
#pragma unroll
    for (int j = 0; j < 4; ++j) {
      const int row = (tid >> 1) + 128 * j;
      wm[j] = ld128(rWM, row * 5376 + chunk * 32 + (tid & 1) * 16);
      cv[j] = __builtin_amdgcn_raw_buffer_load_b16(rC, row * 2, 0, 0);
    }
  }
  v4u wkv[14], wq[7];
#pragma unroll
  for (int i = 0; i < 14; ++i) wkv[i] = ld128(rWQKV, (32 * i + (tid >> 3)) * 2688 + (1 + ((tid >> 2) & 1)) * 896 + hd * 56 + (tid & 3) * 16);
#pragma unroll
  for (int i = 0; i < 7; ++i) wq[i] = ld128(rWQKV, (64 * i + (tid >> 2)) * 2688 + hd * 56 + (tid & 3) * 16);
  const v2u wph = ld64(rWPHI, ((tid >> 3) < 28 && (tid & 7) < 7) ? (hd * 784 + (tid >> 3) * 28 + (tid & 7) * 4) * 2 : 0x7ffffff0);
  if (lane < 56) {
#pragma unroll
    for (int r = 0; r < 32; ++r)
      dma16((lane * 16) ^ (((r >> 1) & 7) * 16), rX, (tt * 128 + w * 32 + r) * 896, lds0 + L_XS + w * 28672 + r * 896);
  }
  const u64 E = ((u64)__builtin_amdgcn_readfirstlane(ep.y) << 32) | (u32)__builtin_amdgcn_readfirstlane(ep.x);
  const u64 tag = E + 1;
  const u32 tlo = (u32)tag, thi = (u32)(tag >> 32);
  const u32 glo = tlo << 16, ghi = (u32)(tag >> 16);

  if (p0) {
    float acc[1][8] = {};
#pragma unroll
    for (int j = 0; j < 4; ++j) {
      const float c = bflo(cv[j]);
      const float s = rbf(c * __builtin_amdgcn_rcpf(1.f + __builtin_amdgcn_exp2f(-1.4426950408889634f * c)));
#pragma unroll
      for (int e = 0; e < 4; ++e) { acc[0][2 * e] = fmaf(s, bflo(wm[j][e]), acc[0][2 * e]); acc[0][2 * e + 1] = fmaf(s, bfhi(wm[j][e]), acc[0][2 * e + 1]); }
    }
    gemv_reduce_store<1>(L, acc, w, lane);
    BAR();
    if (w == 0) {
      const int rep = lane >> 3, pr = lane & 7;
      float v0 = 0.f, v1 = 0.f;
#pragma unroll
      for (int gI = 0; gI < 16; ++gI) { f32x2 a = L.rf2(L_P0S + gI * 64 + pr * 8); v0 += a.x; v1 += a.y; }
      v0 = rbf(v0); v1 = rbf(v1);
      if ((chunk >= 28 && chunk < 56) || (chunk >= 112 && chunk < 140)) { v0 = 1.f + v0; v1 = 1.f + v1; }
      const u32 pk = pk2(v0, v1);
      v4u gr = {glo | (pk & 0xffffu), ghi, glo | (pk >> 16), ghi};
      st128c(rWS, WS_MODG + rep * MODG_REP + (chunk * 16 + 2 * pr) * 8, gr);
    }
  }
  STAMP(1);

  L.w64(L_WPH + (tid >> 3) * 64 + (tid & 7) * 8, wph);
  VMW(0);
  v4u X[28];
  {
    const u32 gsw = (t32 >> 1) & 7;
    u32 xb[4];
#pragma unroll
    for (int j = 0; j < 4; ++j) xb[j] = L_XS + w * 28672 + t32 * 896 + (((2 * j + h) ^ gsw) * 16);
#pragma unroll
    for (int s = 0; s < 28; ++s) X[s] = L.r128(xb[s & 3] + (s >> 2) * 128);
  }
  float rstd1, mr1;
  {
    float s0 = 0.f, s1 = 0.f, q0 = 0.f, q1 = 0.f;
#pragma unroll
    for (int s = 0; s < 28; ++s) {
      s0 = dot2(X[s][0], 0x3f803f80u, s0); s1 = dot2(X[s][1], 0x3f803f80u, s1);
      s0 = dot2(X[s][2], 0x3f803f80u, s0); s1 = dot2(X[s][3], 0x3f803f80u, s1);
      q0 = dot2(X[s][0], X[s][0], q0); q1 = dot2(X[s][1], X[s][1], q1);
      q0 = dot2(X[s][2], X[s][2], q0); q1 = dot2(X[s][3], X[s][3], q1);
    }
    const float m = half_sum(s0 + s1) * (1.f / 448.f);
    const float var = fmaxf(half_sum(q0 + q1) * (1.f / 448.f) - m * m, 0.f);
    rstd1 = __builtin_amdgcn_rsqf(var + 1e-5f);
    mr1 = -m * rstd1;
    if (h == 0) { f32x2 st = {rstd1, mr1}; L.wf2(L_ST1 + (w * 32 + t32) * 8, st); }
  }
  STAMP(2);

  {
    const u32 o0 = WS_MODG + xq * MODG_REP + tid * 16;
    const bool act = tid < 224;
    v4u a, b;
    for (int spins = 0;; ++spins) {
      asm volatile("" ::: "memory");
      a = ld128c(rWS, o0); b = ld128c(rWS, o0 + 448 * 8);
      const bool ok = !act || ((a.x & 0xffff0000u) == glo && a.y == ghi && (a.z & 0xffff0000u) == glo && a.w == ghi &&
                               (b.x & 0xffff0000u) == glo && b.y == ghi && (b.z & 0xffff0000u) == glo && b.w == ghi);
      if (__all(ok)) break;
      if (spins > SPIN_CAP) { st32c(rWS, WS_TMO, 1); break; }
      __builtin_amdgcn_s_sleep(1);
    }
    if (act) { f32x4 v = {bflo(b.x), bflo(a.x), bflo(b.z), bflo(a.z)}; L.wf4(L_T1 + tid * 16, v); }
  }
  BAR();
  STAMP(3);

  {
    float cs8[8] = {}, bs8[8] = {};
#pragma unroll
    for (int i = 0; i < 14; ++i) {
      const int row = 32 * i + (tid >> 3);
      const f32x2 gs = L.rf2(L_T1 + row * 8);
      v4u o;
#pragma unroll
      for (int e = 0; e < 4; ++e) {
        const float a = bflo(wkv[i][e]), b = bfhi(wkv[i][e]);
        const float ga_ = a * gs.x, gb_ = b * gs.x;
        o[e] = pk2(ga_, gb_);
        cs8[2 * e] += ga_; cs8[2 * e + 1] += gb_;
        bs8[2 * e] = fmaf(a, gs.y, bs8[2 * e]); bs8[2 * e + 1] = fmaf(b, gs.y, bs8[2 * e + 1]);
      }
      L.w128(L_WKV + row * 128 + (((tid & 7) ^ (((row >> 1) & 1) << 2)) * 16), o);
    }
#pragma unroll
    for (int j = 0; j < 8; ++j) { cs8[j] = dpp_add<ROR8>(cs8[j]); bs8[j] = dpp_add<ROR8>(bs8[j]); }
    if ((lane & 8) == 0) {
      const u32 base = L_CSP + (w * 4 + (lane >> 4)) * 512 + (lane & 7) * 32;
      f32x4 a = {cs8[0], cs8[1], cs8[2], cs8[3]}, b = {cs8[4], cs8[5], cs8[6], cs8[7]};
      f32x4 c = {bs8[0], bs8[1], bs8[2], bs8[3]}, d = {bs8[4], bs8[5], bs8[6], bs8[7]};
      L.wf4(base, a); L.wf4(base + 16, b); L.wf4(base + 256, c); L.wf4(base + 272, d);
    }
  }
  {
    float cs8[8] = {}, bs8[8] = {};
#pragma unroll
    for (int i = 0; i < 7; ++i) {
      const int row = 64 * i + (tid >> 2);
      const f32x2 gs = L.rf2(L_T1 + row * 8);
      v4u o;
#pragma unroll
      for (int e = 0; e < 4; ++e) {
        const float a = bflo(wq[i][e]), b = bfhi(wq[i][e]);
        const float ga_ = a * gs.x, gb_ = b * gs.x;
        o[e] = pk2(ga_, gb_);
        cs8[2 * e] += ga_; cs8[2 * e + 1] += gb_;
        bs8[2 * e] = fmaf(a, gs.y, bs8[2 * e]); bs8[2 * e + 1] = fmaf(b, gs.y, bs8[2 * e + 1]);
      }
      L.w128(L_WQ + row * 64 + (tid & 3) * 16, o);
    }
#pragma unroll
    for (int j = 0; j < 8; ++j) { cs8[j] = dpp_add<ROR8>(dpp_add<ROR4>(cs8[j])); bs8[j] = dpp_add<ROR8>(dpp_add<ROR4>(bs8[j])); }
    if ((lane & 12) == 0) {
      const u32 base = L_CSQ + (w * 4 + (lane >> 4)) * 256 + (lane & 3) * 32;
      f32x4 a = {cs8[0], cs8[1], cs8[2], cs8[3]}, b = {cs8[4], cs8[5], cs8[6], cs8[7]};
      f32x4 c = {bs8[0], bs8[1], bs8[2], bs8[3]}, d = {bs8[4], bs8[5], bs8[6], bs8[7]};
      L.wf4(base, a); L.wf4(base + 16, b); L.wf4(base + 128, c); L.wf4(base + 144, d);
    }
  }
  BAR();
  if (tid < 192) {
    const int kind = tid >= 96, col = tid - 96 * kind;
    float a = 0.f;
    if (col < 64) {
#pragma unroll
      for (int gI = 0; gI < 16; ++gI) a += L.rf(L_CSP + gI * 512 + kind * 256 + col * 4);
    } else {
#pragma unroll
      for (int gI = 0; gI < 16; ++gI) a += L.rf(L_CSQ + gI * 256 + kind * 128 + (col - 64) * 4);
    }
    L.wf(L_CSF + tid * 4, a);
  }
  STAMP(4);

  f32x16 accK = {}, accV = {};
  {
    const u32 sw = ((q4 >> 1) & 1) << 2;
    const u32 rowb = L_WKV + (8 * h + q4) * 128 + (p4 & 1) * 8;
    const u32 bk = rowb + (((2 * g16 + (p4 >> 1)) ^ sw) * 16), bv = rowb + (((4 + 2 * g16 + (p4 >> 1)) ^ sw) * 16);
    v4u fk[2], fv[2];
    fk[0] = L.tr2(bk, bk + 512); fv[0] = L.tr2(bv, bv + 512);
#pragma unroll
    for (int s = 0; s < 28; ++s) {
      accK = mfma(fk[s & 1], X[s], accK);
      if (s + 1 < 28) fk[(s + 1) & 1] = L.tr2(bk + (s + 1) * 2048, bk + (s + 1) * 2048 + 512);
      SCHED();
      accV = mfma(X[s], fv[s & 1], accV);
      if (s + 1 < 28) fv[(s + 1) & 1] = L.tr2(bv + (s + 1) * 2048, bv + (s + 1) * 2048 + 512);
      SCHED();
    }
  }
  BAR();
  STAMP(5);
  v4u PH[2];
  {
    const u32 b = L_WPH + (4 * h + q4) * 64 + g16 * 32 + p4 * 8;
    PH[0] = L.tr2(b, b + 8 * 64); PH[1] = L.tr2(b + 16 * 64, b + 24 * 64);
  }
  {
    u32 kb[8];
#pragma unroll
    for (int qq = 0; qq < 4; ++qq) {
      const f32x4 c4 = L.rf4(L_CSF + (8 * qq + 4 * h) * 4), b4 = L.rf4(L_CSF + 384 + (8 * qq + 4 * h) * 4);
      float v[4];
#pragma unroll
      for (int e = 0; e < 4; ++e) v[e] = fmaf(accK[4 * qq + e], rstd1, fmaf(mr1, c4[e], b4[e]));
      kb[2 * qq] = pk2(v[0], v[1]); kb[2 * qq + 1] = pk2(v[2], v[3]);
    }
    f32x16 pk = {};
    { v4u b0 = {kb[0], kb[1], kb[2], kb[3]}, b1 = {kb[4], kb[5], kb[6], kb[7]}; pk = mfma(PH[0], b0, pk); pk = mfma(PH[1], b1, pk); }
    {
      const float cv_ = L.rf(L_CSF + (32 + t32) * 4), bv_ = L.rf(L_CSF + 384 + (32 + t32) * 4);
      u32 vb[8];
#pragma unroll
      for (int qq = 0; qq < 4; ++qq) {
        const u32 so = L_ST1 + (w * 32 + 8 * qq + 4 * h) * 8;
        const f32x4 s01 = L.rf4(so), s23 = L.rf4(so + 16);
        float v[4];
        v[0] = fmaf(accV[4 * qq + 0], s01[0], fmaf(s01[1], cv_, bv_));
        v[1] = fmaf(accV[4 * qq + 1], s01[2], fmaf(s01[3], cv_, bv_));
        v[2] = fmaf(accV[4 * qq + 2], s23[0], fmaf(s23[1], cv_, bv_));
        v[3] = fmaf(accV[4 * qq + 3], s23[2], fmaf(s23[3], cv_, bv_));
#pragma unroll
        for (int e = 0; e < 4; ++e) v[e] = t32 < 28 ? v[e] : (t32 == 28 ? 1.f : 0.f);
        vb[2 * qq] = pk2(v[0], v[1]); vb[2 * qq + 1] = pk2(v[2], v[3]);
      }
      v4u a = {vb[0], vb[1], vb[2], vb[3]}, b = {vb[4], vb[5], vb[6], vb[7]};
      L.w128(L_VFR + w * 2048 + lane * 16, a); L.w128(L_VFR + w * 2048 + 1024 + lane * 16, b);
    }
    u32 P1[8], P2[8];
    feature_map(pk, h, P1, P2);
    const u32 swt = (t32 & 7) | ((((t32 >> 3) ^ (t32 >> 1)) & 1) << 3);
    const u32 rb = L_PHI + w * 4096 + t32 * 128;
#pragma unroll
    for (int qq = 0; qq < 4; ++qq) {
      v2u a = {P1[2 * qq], P1[2 * qq + 1]}, b = {P2[2 * qq], P2[2 * qq + 1]};
      L.w64(rb + (((2 * qq + h) ^ swt) * 8), a);
      L.w64(rb + (((8 + 2 * qq + h) ^ swt) * 8), b);
    }
  }
  BAR();
  STAMP(6);
  if (w < 2) {
    f32x16 kv = {};
#pragma unroll
    for (int ww = 0; ww < 4; ++ww)
#pragma unroll
      for (int s = 0; s < 2; ++s) {
        u32 ad[2];
#pragma unroll
        for (int u = 0; u < 2; ++u) {
          const u32 t = 16 * s + 8 * u + 4 * h + q4;
          const u32 sw_ = (t & 7) | ((((t >> 3) ^ (t >> 1)) & 1) << 3);
          ad[u] = L_PHI + ww * 4096 + t * 128 + (((8 * w + 4 * g16 + p4) ^ sw_) * 8);
        }
        const v4u a = L.tr2(ad[0], ad[1]);
        const v4u b = L.r128(L_VFR + ww * 2048 + s * 1024 + lane * 16);
        kv = mfma(a, b, kv);
      }
    v4u o0 = {pk2(kv[0], kv[1]), pk2(kv[2], kv[3]), pk2(kv[4], kv[5]), pk2(kv[6], kv[7])};
    v4u o1 = {pk2(kv[8], kv[9]), pk2(kv[10], kv[11]), pk2(kv[12], kv[13]), pk2(kv[14], kv[15])};
    const u32 ob = WS_KVP + (hd * 16 + tt) * 4096 + w * 2048 + lane * 16;
    st128c(rWS, ob, o0); st128c(rWS, ob + 1024, o1);
  }
  STAMP(7);
  f32x16 accQ = {};
  {
    const u32 bq = L_WQ + (8 * h + q4) * 64 + g16 * 32 + p4 * 8;
    v4u fq[2];
    fq[0] = L.tr2(bq, bq + 256);
#pragma unroll
    for (int s = 0; s < 28; ++s) {
      accQ = mfma(fq[s & 1], X[s], accQ);
      if (s + 1 < 28) fq[(s + 1) & 1] = L.tr2(bq + (s + 1) * 1024, bq + (s + 1) * 1024 + 256);
      SCHED();
    }
  }
  VMW(0);
  BAR();
  if (tid == 0) set_flag(rWS, FL_KV + hd * 16 + tt, tlo, thi);
  STAMP(8);
  if (p24) {
    const u32 rr = lane >> 3, c = (lane & 7) ^ (((rr >> 1) & 1) << 2);
    const u32 voff = rr * 896 + cs * 128 + c * 16;
#pragma unroll
    for (int i = 0; i < 14; ++i) { const int id = i * 4 + w; dma16(voff, rWO, id * 8 * 896, lds0 + L_WO + id * 1024); }
  }
  v4u mg[4];
#pragma unroll
  for (int j = 0; j < 4; ++j) mg[j] = ld128c(rWS, WS_MODG + xq * MODG_REP + 896 * 8 + (j * 256 + tid) * 16);
  v4u wc[4];
  if (p0c) {
    const int c2 = chunk;
    const rsrc_t rW = c2 < 112 ? rWG : rWU;
    const int col0 = 16 * (c2 < 112 ? c2 : c2 - 112);
#pragma unroll
    for (int j = 0; j < 4; ++j) wc[j] = ld128(rW, ((tid >> 1) + 128 * j) * 3584 + col0 * 2 + (tid & 1) * 16);
  }
  u32 Q1[8], Q2[8];
  {
    u32 qb[8];
#pragma unroll
    for (int qq = 0; qq < 4; ++qq) {
      const f32x4 c4 = L.rf4(L_CSF + (64 + 8 * qq + 4 * h) * 4), b4 = L.rf4(L_CSF + 384 + (64 + 8 * qq + 4 * h) * 4);
      float v[4];
#pragma unroll
      for (int e = 0; e < 4; ++e) v[e] = fmaf(accQ[4 * qq + e], rstd1, fmaf(mr1, c4[e], b4[e]));
      qb[2 * qq] = pk2(v[0], v[1]); qb[2 * qq + 1] = pk2(v[2], v[3]);
    }
    f32x16 pq = {};
    v4u b0 = {qb[0], qb[1], qb[2], qb[3]}, b1 = {qb[4], qb[5], qb[6], qb[7]};
    pq = mfma(PH[0], b0, pq); pq = mfma(PH[1], b1, pq);
    feature_map(pq, h, Q1, Q2);
  }
  STAMP(9);
  {
    for (int spins = 0;; ++spins) {
      bool ok = true;
#pragma unroll
      for (int j = 0; j < 4; ++j) {
        const bool act = j * 256 + tid < 896;
        ok &= !act || ((mg[j].x & 0xffff0000u) == glo && mg[j].y == ghi && (mg[j].z & 0xffff0000u) == glo && mg[j].w == ghi);
      }
      if (__all(ok)) break;
      if (spins > SPIN_CAP) { st32c(rWS, WS_TMO, 2); break; }
      asm volatile("" ::: "memory");
#pragma unroll
      for (int j = 0; j < 4; ++j) mg[j] = ld128c(rWS, WS_MODG + xq * MODG_REP + 896 * 8 + (j * 256 + tid) * 16);
    }
#pragma unroll
    for (int j = 0; j < 4; ++j)
      if (j * 256 + tid < 896) { f32x2 v = {bflo(mg[j].x), bflo(mg[j].z)}; L.wf2(L_T2 + (j * 256 + tid) * 8, v); }
  }
  if (w == 0) wait_flags(rWS, WS_FLAG + (FL_KV + hd * 16 + (lane & 15)) * 256, lane < 16, tlo, thi, 3);
  BAR();
  STAMP(10);
  {
    v4u pp[16];
#pragma unroll
    for (int i = 0; i < 16; ++i) pp[i] = ld128c(rWS, WS_KVP + (hd * 16 + i) * 4096 + w * 1024 + lane * 16);
    float s[8] = {};
    u32 selLo, selHi;
    asm("s_mov_b32 %0, 0x3f80" : "=s"(selLo));
    asm("s_mov_b32 %0, 0x3f800000" : "=s"(selHi));
#pragma unroll
    for (int i = 0; i < 16; ++i)
#pragma unroll
      for (int e = 0; e < 4; ++e) { s[2 * e] = dot2(pp[i][e], selLo, s[2 * e]); s[2 * e + 1] = dot2(pp[i][e], selHi, s[2 * e + 1]); }
    v4u o = {pk2(s[0], s[1]), pk2(s[2], s[3]), pk2(s[4], s[5]), pk2(s[6], s[7])};
    L.w128(L_KVR + w * 1024 + lane * 16, o);
  }
  BAR();
  STAMP(11);
  {
    f32x16 o = {};
    v4u b0 = {Q1[0], Q1[1], Q1[2], Q1[3]}, b1 = {Q1[4], Q1[5], Q1[6], Q1[7]}, b2 = {Q2[0], Q2[1], Q2[2], Q2[3]}, b3 = {Q2[4], Q2[5], Q2[6], Q2[7]};
    o = mfma(L.r128(L_KVR + lane * 16), b0, o);
    o = mfma(L.r128(L_KVR + 1024 + lane * 16), b1, o);
    o = mfma(L.r128(L_KVR + 2048 + lane * 16), b2, o);
    o = mfma(L.r128(L_KVR + 3072 + lane * 16), b3, o);
    const float den = half_hi(o[12]) + 1e-6f;
    const float rd = __builtin_amdgcn_rcpf(den);
    float v[16];
#pragma unroll
    for (int r = 0; r < 16; ++r) v[r] = o[r] * rd;
#pragma unroll
    for (int r = 12; r < 16; ++r) v[r] = h ? 0.f : v[r];
    v4u o0 = {pk2(v[0], v[1]), pk2(v[2], v[3]), pk2(v[4], v[5]), pk2(v[6], v[7])};
    v4u o1 = {pk2(v[8], v[9]), pk2(v[10], v[11]), pk2(v[12], v[13]), pk2(v[14], v[15])};
    const u32 ob = WS_OFR + ((tt * 4 + w) * 32 + hd * 2) * 1024 + lane * 16;
    st128c(rWS, ob, o0); st128c(rWS, ob + 1024, o1);
  }
  STAMP(12);
  if (p0c) {
    float acc[2][8] = {};
#pragma unroll
    for (int j = 0; j < 4; ++j) {
      const int row = (tid >> 1) + 128 * j;
      const int rc = row < 448 ? row : 0;
      const float g2 = L.rf(L_T2 + (2 * 448 + rc) * 4), sb = L.rf(L_T2 + (448 + rc) * 4);
#pragma unroll
      for (int e = 0; e < 4; ++e) {
        const float a = bflo(wc[j][e]), b = bfhi(wc[j][e]);
        acc[0][2 * e] = fmaf(g2, a, acc[0][2 * e]); acc[0][2 * e + 1] = fmaf(g2, b, acc[0][2 * e + 1]);
        acc[1][2 * e] = fmaf(sb, a, acc[1][2 * e]); acc[1][2 * e + 1] = fmaf(sb, b, acc[1][2 * e + 1]);
      }
    }
    gemv_reduce_store<2>(L, acc, w, lane);
  }
  if (p24 && tid < 192) {
    const int kind = tid >> 6, c = tid & 63;
    const int src = kind == 0 ? 0 : (kind == 1 ? 2 : 3);
    L.wf(L_TGA + tid * 4, L.rf(L_T2 + (src * 448 + cs * 64 + c) * 4));
  }
  VMW(0);
  BAR();
  if (tid == 0) set_flag(rWS, FL_O + tt * 16 + hd, tlo, thi);
  if (p0c && w == 1) {
    const int c2 = chunk, mat = c2 >= 112, col0 = 16 * (c2 - 112 * mat);
    float a = 0.f;
    if (lane < 32) {
#pragma unroll
      for (int gI = 0; gI < 16; ++gI) a += L.rf(L_P0S + gI * 128 + lane * 4);
      st32c(rWS, WS_CSB + (((lane >> 4) * 2 + mat) * 1792 + col0 + (lane & 15)) * 4, __float_as_uint(a));
    }
  }
  STAMP(13);
  BAR();
  const int fs = hd;
  u32 vB[3];
#pragma unroll
  for (int j = 0; j < 3; ++j) { const u32 pI = 64 * j + lane; vB[j] = (pI / 6) * 3584 + fs * 224 + 128 + (pI % 6) * 16; }
  const u32 rrA = lane >> 3, pA = (lane & 7) >> 1, eA = lane & 1;
  const u32 vAG = rrA * 3584 + fs * 224 + ((pA ^ ((rrA >> 1) & 1)) * 32) + eA * 16;
  const u32 vAU = rrA * 3584 + fs * 224 + ((pA ^ 2 ^ ((rrA >> 1) & 1)) * 32) + eA * 16;
  {
#pragma unroll
    for (int i = 0; i < 11; ++i) {
      const int id = i * 4 + w;
      if (id < 42) {
        const int mat = id >= 21, r3 = id - 21 * mat, rnd = r3 / 3, j = r3 - 3 * rnd;
        const u32 vo = j == 0 ? vB[0] : (j == 1 ? vB[1] : vB[2]);
        if (mat) dma16(vo, rWU, rnd * 32 * 3584, lds0 + L_B1U + r3 * 1024);
        else dma16(vo, rWG, rnd * 32 * 3584, lds0 + L_B1G + r3 * 1024);
      }
    }
#pragma unroll
    for (int i = 0; i < 10; ++i) {
      const int rg = 16 + i * 4 + w;
      dma16(vAU, rWU, rg * 8 * 3584, lds0 + L_AU + rg * 1024);
    }
  }
  STAMP(14);

  u32 x1p[8] = {};
  const int tt2 = w & 1, kh = w >> 1;
  if (p24) {
    v2u xr[4];
    const u32 T32 = t64 * 2 + tt2;
#pragma unroll
    for (int qq = 0; qq < 4; ++qq) xr[qq] = ld64(rX, (T32 * 32 + t32) * 896 + (cs * 64 + kh * 32 + 8 * qq + 4 * h) * 2);
    if (w == 0) wait_flags(rWS, WS_FLAG + (FL_O + (t64 >> 1) * 16 + (lane & 15)) * 256, lane < 16, tlo, thi, 4);
    VMW(0);
    BAR();
    if (bid == 0 && tid == 0) { v2u t = {tlo, thi}; st64c(rWS, WS_EPOCH, t); }
    if (tid == 64) set_flag(rWS, FL_C + chunk, tlo, thi);
    STAMP(15);
    v4u O[16];
#pragma unroll
    for (int i = 0; i < 16; ++i) O[i] = ld128c(rWS, WS_OFR + (T32 * 32 + kh * 16 + i) * 1024 + lane * 16);
    f32x16 acc[2] = {};
    {
      const u32 sw = ((q4 >> 1) & 1) << 2;
      const u32 cb0 = (((2 * g16 + (p4 >> 1)) ^ sw) * 16) + (p4 & 1) * 8, cb1 = (((4 + 2 * g16 + (p4 >> 1)) ^ sw) * 16) + (p4 & 1) * 8;
      const u32 rb = L_WO + kh * 8 * 28 * 128 + q4 * 128;
      const u32 bh0 = rb + 4 * h * 128 + cb0, bh1 = rb + 4 * h * 128 + cb1, bn0 = rb + cb0, bn1 = rb + cb1;
      auto rd = [&](int i, int ct) -> v4u {
        const int hh = i >> 1, s = i & 1;
        const u32 o0 = (28 * hh + 16 * s) * 128, o1 = o0 + 8 * 128;
        const u32 b = ct ? bh1 : bh0, bn = ct ? bn1 : bn0;
        return L.tr2(b + o0, (s ? bn : b) + o1);
      };
      v4u f[2][2];
      f[0][0] = rd(0, 0); f[0][1] = rd(0, 1);
#pragma unroll
      for (int i = 0; i < 16; ++i) {
        acc[0] = mfma(f[i & 1][0], O[i], acc[0]);
        if (i + 1 < 16) f[(i + 1) & 1][0] = rd(i + 1, 0);
        SCHED();
        acc[1] = mfma(f[i & 1][1], O[i], acc[1]);
        if (i + 1 < 16) f[(i + 1) & 1][1] = rd(i + 1, 1);
        SCHED();
      }
    }
    {
      const f32x16 snd = kh ? acc[0] : acc[1];
      const u32 xb = L_XCH + w * 4096 + lane * 16;
#pragma unroll
      for (int qq = 0; qq < 4; ++qq) { f32x4 v = {snd[4 * qq], snd[4 * qq + 1], snd[4 * qq + 2], snd[4 * qq + 3]}; L.wf4(xb + qq * 1024, v); }
    }
    BAR();
    {
      const f32x16 mine = kh ? acc[1] : acc[0];
      const u32 xb = L_XCH + (w ^ 2) * 4096 + lane * 16;
      float x1[16], a2[16];
      float sum = 0.f;
#pragma unroll
      for (int qq = 0; qq < 4; ++qq) {
        const f32x4 o = L.rf4(xb + qq * 1024);
        const f32x4 ga4 = L.rf4(L_TGA + (kh * 32 + 8 * qq + 4 * h) * 4);
        const float xv[4] = {bflo(xr[qq].x), bfhi(xr[qq].x), bflo(xr[qq].y), bfhi(xr[qq].y)};
#pragma unroll
        for (int e = 0; e < 4; ++e) x1[4 * qq + e] = fmaf(ga4[e], mine[4 * qq + e] + o[e], xv[e]);
        x1p[2 * qq] = pk2(x1[4 * qq], x1[4 * qq + 1]); x1p[2 * qq + 1] = pk2(x1[4 * qq + 2], x1[4 * qq + 3]);
        x1[4 * qq] = bflo(x1p[2 * qq]); x1[4 * qq + 1] = bfhi(x1p[2 * qq]); x1[4 * qq + 2] = bflo(x1p[2 * qq + 1]); x1[4 * qq + 3] = bfhi(x1p[2 * qq + 1]);
        const f32x4 g24 = L.rf4(L_TG2 + (kh * 32 + 8 * qq + 4 * h) * 4);
#pragma unroll
        for (int e = 0; e < 4; ++e) { a2[4 * qq + e] = x1[4 * qq + e] * g24[e]; sum += x1[4 * qq + e]; }
      }
      const float mean = half_sum(sum) * (1.f / 32.f);
      float m2 = 0.f;
#pragma unroll
      for (int r = 0; r < 16; ++r) { const float d = x1[r] - mean; m2 = fmaf(d, d, m2); }
      m2 = half_sum(m2);
      v4u o0 = {pk2(a2[0], a2[1]), pk2(a2[2], a2[3]), pk2(a2[4], a2[5]), pk2(a2[6], a2[7])};
      v4u o1 = {pk2(a2[8], a2[9]), pk2(a2[10], a2[11]), pk2(a2[12], a2[13]), pk2(a2[14], a2[15])};
      const u32 ob = WS_X1F + (T32 * 28 + cs * 4 + kh * 2) * 1024 + lane * 16;
      st128c(rWS, ob, o0); st128c(rWS, ob + 1024, o1);
      if (h == 0) { v2u st = {__float_as_uint(mean), __float_as_uint(m2)}; st64c(rWS, WS_STAT + ((T32 * 14 + cs * 2 + kh) * 32 + t32) * 8, st); }
    }
    VMW(0);
    BAR();
    if (tid == 0) set_flag(rWS, FL_X + t64 * 7 + cs, tlo, thi);
  } else {
    VMW(0);
    BAR();
  }
  STAMP(16);
#pragma unroll
  for (int i = 0; i < 18; ++i) {
    const int id = i * 4 + w, mat = id >= 56, rg = id - 56 * mat;
    if (mat) dma16(vAU, rWU, rg * 8 * 3584, lds0 + L_AU + rg * 1024);
    else dma16(vAG, rWG, rg * 8 * 3584, lds0 + L_AG + rg * 1024);
  }
  if (w == 0) {
    u32 fo;
    if (lane < 14) fo = FL_X + (tt * 2 + lane / 7) * 7 + lane % 7;
    else fo = FL_C + (lane < 21 ? 7 * fs + (lane - 14) : 112 + 7 * fs + (lane - 21));
    wait_flags(rWS, WS_FLAG + fo * 256, lane < 28, tlo, thi, 5);
  }
  VMW(0);
  BAR();
  STAMP(17);
  v4u B[28];
  const u32 T32p = tt * 4 + w;
#pragma unroll
  for (int s = 0; s < 28; ++s) B[s] = ld128c(rWS, WS_X1F + (T32p * 28 + s) * 1024 + lane * 16);
  float rstd2, mr2;
  {
    v2u st[14];
#pragma unroll
    for (int i = 0; i < 14; ++i) st[i] = ld64c(rWS, WS_STAT + ((T32p * 14 + i) * 32 + t32) * 8);
    if (tid < 112) {
      const int kind = tid / 28, j = tid % 28;
      const v4u v = ld128c(rWS, WS_CSB + (kind * 1792 + fs * 112 + j * 4) * 4);
      L.w128(L_TCS + (j >> 2) * 256 + kind * 64 + (j & 3) * 16, v);
    }
    float mean = 0.f, m2 = 0.f;
#pragma unroll
    for (int i = 0; i < 14; ++i) mean += __uint_as_float(st[i].x);
    mean *= (1.f / 14.f);
#pragma unroll
    for (int i = 0; i < 14; ++i) { const float d = __uint_as_float(st[i].x) - mean; m2 += __uint_as_float(st[i].y) + 32.f * d * d; }
    rstd2 = __builtin_amdgcn_rsqf(m2 * (1.f / 448.f) + 1e-5f);
    mr2 = -mean * rstd2;
  }
  auto act_store = [&](const f32x16& a, int tile) {
    float v[8];
#pragma unroll
    for (int qq = 0; qq < 2; ++qq) {
      const u32 tb = L_TCS + tile * 256 + (8 * qq + 4 * h) * 4;
      const f32x4 cg = L.rf4(tb), cu = L.rf4(tb + 64), bg = L.rf4(tb + 128), bu = L.rf4(tb + 192);
#pragma unroll
      for (int e = 0; e < 4; ++e) {
        const float g = fmaf(a[4 * qq + e], rstd2, fmaf(mr2, cg[e], bg[e]));
        const float u = fmaf(a[8 + 4 * qq + e], rstd2, fmaf(mr2, cu[e], bu[e]));
        v[4 * qq + e] = g * u;
      }
    }
    v4u o = {pk2(v[0], v[1]), pk2(v[2], v[3]), pk2(v[4], v[5]), pk2(v[6], v[7])};
    st128c(rWS, WS_ACT + (T32p * 112 + fs * 7 + tile) * 1024 + lane * 16, o);
  };
  {
    f32x16 acc[4] = {};
    u32 bA[4];
#pragma unroll
    for (int i = 0; i < 4; ++i) bA[i] = (g16 ? L_AU : L_AG) + (4 * h + q4) * 128 + ((i ^ ((q4 >> 1) & 1) ^ (g16 * 2)) * 32) + p4 * 8;
    v4u f[2][4];
#pragma unroll
    for (int i = 0; i < 4; ++i) f[0][i] = L.tr2(bA[i], bA[i] + 1024);
#pragma unroll
    for (int s = 0; s < 28; ++s) {
#pragma unroll
      for (int i = 0; i < 4; ++i) {
        acc[i] = mfma(f[s & 1][i], B[s], acc[i]);
        if (s + 1 < 28) f[(s + 1) & 1][i] = L.tr2(bA[i] + (s + 1) * 2048, bA[i] + (s + 1) * 2048 + 1024);
        SCHED();
      }
    }
    STAMP(18);
    BAR();
#pragma unroll
    for (int i = 0; i < 11; ++i) {
      const int id = i * 4 + w;
      if (id < 42) {
        const int mat = id >= 21, r3 = id - 21 * mat, rnd = r3 / 3, j = r3 - 3 * rnd;
        const u32 vo = j == 0 ? vB[0] : (j == 1 ? vB[1] : vB[2]);
        if (mat) dma16(vo, rWU, (224 + rnd * 32) * 3584, lds0 + L_B2U + r3 * 1024);
        else dma16(vo, rWG, (224 + rnd * 32) * 3584, lds0 + L_B2G + r3 * 1024);
      }
    }
#pragma unroll
    for (int i = 0; i < 4; ++i) act_store(acc[i], i);
  }
  STAMP(19);
  {
    f32x16 acc[3] = {};
    const u32 b1 = (g16 ? L_B1U : L_B1G) + (4 * h + q4) * 96 + p4 * 8;
    const u32 b2 = (g16 ? L_B2U : L_B2G) + (4 * h + q4) * 96 + p4 * 8;
    auto rd = [&](int s, int i) -> v4u {
      const u32 b = s < 14 ? b1 : b2;
      const u32 o = ((s < 14 ? s : s - 14) * 16) * 96 + i * 32;
      return L.tr2(b + o, b + o + 8 * 96);
    };
    v4u f[2][3];
#pragma unroll
    for (int i = 0; i < 3; ++i) f[0][i] = rd(0, i);
#pragma unroll
    for (int s = 0; s < 28; ++s) {
      if (s == 13) { VMW(0); BAR(); }
#pragma unroll
      for (int i = 0; i < 3; ++i) {
        acc[i] = mfma(f[s & 1][i], B[s], acc[i]);
        if (s + 1 < 28) f[(s + 1) & 1][i] = rd(s + 1, i);
        SCHED();
      }
    }
    STAMP(20);
#pragma unroll
    for (int i = 0; i < 3; ++i) act_store(acc[i], 4 + i);
  }
  BAR();
  const u32 rrD = lane >> 3;
  const u32 vD = rrD * 896 + cs * 128 + (((lane & 7) ^ (((rrD >> 1) & 1) << 2)) * 16);
  if (p24) {
#pragma unroll
    for (int i = 0; i < 36; ++i) {
      const int id = i * 4 + w, hf = id >= 72, pc = id - 72 * hf;
      dma16(vD, rWD, (hf * 896 + pc * 8) * 896, lds0 + (hf ? L_WD1 : L_WD0) + pc * 1024);
    }
    VMW(36);
  } else {
    VMW(0);
  }
  BAR();
  if (tid == 0) set_flag(rWS, FL_A + (tt * 16 + fs) * 2, tlo, thi);
  STAMP(21);
  if (p24) {
    if (w == 0) wait_flags(rWS, WS_FLAG + (FL_A + ((t64 >> 1) * 16 + (lane & 15)) * 2) * 256, lane < 16, tlo, thi, 6);
    VMW(0);
    BAR();
    STAMP(22);
    const u32 T32 = t64 * 2 + tt2;
    const u32 ab = WS_ACT + (T32 * 112 + kh * 56) * 1024 + lane * 16;
    constexpr int WIN = 24;
    v4u A_[56];
#pragma unroll
    for (int i = 0; i < WIN; ++i) A_[i] = ld128c(rWS, ab + i * 1024);
    f32x16 acc[2] = {};
    const u32 sw = ((q4 >> 1) & 1) << 2;
    const u32 rb = (kh ? L_WD1 : L_WD0) + (4 * h + q4) * 128 + (p4 & 1) * 8;
    const u32 c0 = rb + (((2 * g16 + (p4 >> 1)) ^ sw) * 16), c1 = rb + (((4 + 2 * g16 + (p4 >> 1)) ^ sw) * 16);
    auto rd = [&](int o, int ct) -> v4u { const u32 b = (ct ? c1 : c0) + (o < 36 ? o : o - 36) * 2048; return L.tr2(b, b + 1024); };
    v4u f[2][2];
    f[0][0] = rd(0, 0); f[0][1] = rd(0, 1);
#pragma unroll
    for (int o = 0; o < 56; ++o) {
      if (o == 20) {
        BAR();
#pragma unroll
        for (int i = 0; i < 20; ++i) {
          const int id = i * 4 + w, hf = id >= 40, pc = id - 40 * hf;
          dma16(vD, rWD, (hf * 896 + (72 + pc) * 8) * 896, lds0 + (hf ? L_WD1 : L_WD0) + pc * 1024);
        }
      }
      if (o == 35) { VMW(0); BAR(); }
      acc[0] = mfma(f[o & 1][0], A_[o], acc[0]);
      if (o + 1 < 56) f[(o + 1) & 1][0] = rd(o + 1, 0);
      SCHED();
      acc[1] = mfma(f[o & 1][1], A_[o], acc[1]);
      if (o + 1 < 56) f[(o + 1) & 1][1] = rd(o + 1, 1);
      if (o + WIN < 56) A_[o + WIN] = ld128c(rWS, ab + (o + WIN) * 1024);
      SCHED();
    }
    STAMP(23);
    BAR();
    {
      const f32x16 snd = kh ? acc[0] : acc[1];
      const u32 xb = w * 4096 + lane * 16;
#pragma unroll
      for (int qq = 0; qq < 4; ++qq) { f32x4 v = {snd[4 * qq], snd[4 * qq + 1], snd[4 * qq + 2], snd[4 * qq + 3]}; L.wf4(xb + qq * 1024, v); }
    }
    BAR();
    {
      const f32x16 mine = kh ? acc[1] : acc[0];
      const u32 xb = (w ^ 2) * 4096 + lane * 16;
      u32 op[8];
#pragma unroll
      for (int qq = 0; qq < 4; ++qq) {
        const f32x4 o = L.rf4(xb + qq * 1024);
        const f32x4 gb4 = L.rf4(L_TGB + (kh * 32 + 8 * qq + 4 * h) * 4);
        const float xv[4] = {bflo(x1p[2 * qq]), bfhi(x1p[2 * qq]), bflo(x1p[2 * qq + 1]), bfhi(x1p[2 * qq + 1])};
        float v[4];
#pragma unroll
        for (int e = 0; e < 4; ++e) v[e] = fmaf(gb4[e], mine[4 * qq + e] + o[e], xv[e]);
        op[2 * qq] = pk2(v[0], v[1]); op[2 * qq + 1] = pk2(v[2], v[3]);
      }
#pragma unroll
      for (int pr = 0; pr < 2; ++pr) {
        auto s0 = __builtin_amdgcn_permlane32_swap(op[4 * pr], op[4 * pr + 2], false, false);
        auto s1 = __builtin_amdgcn_permlane32_swap(op[4 * pr + 1], op[4 * pr + 3], false, false);
        v4u o = {s0[0], s1[0], s0[1], s1[1]};
        __builtin_amdgcn_raw_buffer_store_b128(o, rOUT, (T32 * 32 + t32) * 896 + (cs * 64 + kh * 32 + 16 * pr + 8 * h) * 2, 0, 0);
      }
    }
    STAMP(24);
  }
}

extern "C" void kernel_launch(void* const* d_in, const int* in_sizes, int n_in, void* d_out, int out_size, void* d_ws, size_t ws_size,
                              hipStream_t stream) {
  (void)in_sizes; (void)n_in; (void)out_size; (void)ws_size;
  hipFuncSetAttribute((const void*)fused_block, hipFuncAttributeMaxDynamicSharedMemorySize, LDS_BYTES);
  fused_block<<<256, 256, LDS_BYTES, stream>>>((const u16*)d_in[0], (const u16*)d_in[1], (const u16*)d_in[2], (const u16*)d_in[3],
                                               (const u16*)d_in[5], (const u16*)d_in[6], (const u16*)d_in[7], (const u16*)d_in[8],
                                               (const u16*)d_in[9], (u16*)d_out, (unsigned char*)d_ws);
}
```

```cpp
#include <hip/hip_runtime.h>
#include <stdint.h>

typedef unsigned u32;
typedef unsigned long long u64;
typedef unsigned short u16;
typedef u32 v4u __attribute__((ext_vector_type(4)));
typedef u32 v2u __attribute__((ext_vector_type(2)));
typedef float f32x2 __attribute__((ext_vector_type(2)));
typedef float f32x4 __attribute__((ext_vector_type(4)));
typedef float f32x16 __attribute__((ext_vector_type(16)));
typedef __bf16 bf16x2 __attribute__((ext_vector_type(2)));
typedef __bf16 bf16x8 __attribute__((ext_vector_type(8)));
typedef short s16x4 __attribute__((ext_vector_type(4)));
typedef __amdgpu_buffer_rsrc_t rsrc_t;
#define LAS __attribute__((address_space(3)))
#define DI __device__ __forceinline__

constexpr u32 WS_EPOCH = 0;
constexpr u32 WS_TMO = 256;
constexpr u32 WS_FLAG = 4096;
constexpr u32 FL_KV = 0, FL_O = 256, FL_X = 512, FL_A = 1024;
constexpr u32 WS_MODG = 0x80000;
constexpr u32 MODG_REP = 2688 * 8;
constexpr u32 WS_CSB = 0xB0000;
constexpr u32 WS_STAT = 0xC0000;
constexpr u32 WS_KVP = 0x100000;
constexpr u32 WS_OFR = 0x200000;
constexpr u32 WS_X1F = 0x400000;
constexpr u32 WS_ACT = 0x600000;
constexpr u32 WS_DBG = 0xE00000;
constexpr u32 WS_USED = 0x1000000;

constexpr u32 L_XS = 0;
constexpr u32 L_WKV = 0, L_WQ = 57344;
constexpr u32 L_PHI = 86016, L_VFR = 102400;
constexpr u32 L_T1 = 114688;
constexpr u32 L_ST1 = 118272;
constexpr u32 L_WPH = 119296;
constexpr u32 L_P0S = 121344;
constexpr u32 L_CSP = 123392;
constexpr u32 L_CSQ = 131584;
constexpr u32 L_CSF = 135680;
constexpr u32 L_WO = 0, L_XCH = 57344, L_P0C = 57344;
constexpr u32 L_AG = 0, L_AU = 57344;
constexpr u32 L_B1G = 114688, L_B1U = 136320;
constexpr u32 L_B2G = 0, L_B2U = 57472;
constexpr u32 L_KVR = 157824, L_TCS = 157824;
constexpr u32 L_TGA = 161920, L_TG2 = 162176, L_TGB = 162432;
constexpr u32 L_WD0 = 0, L_WD1 = 73728;
constexpr u32 LDS_BYTES = 163840;

constexpr int SPIN_CAP = 40000;

template <int V> struct IC { static constexpr int value = V; };
template <int I, int N, class F> DI void sfor(F&& f) { if constexpr (I < N) { f(IC<I>{}); sfor<I + 1, N>(f); } }

DI rsrc_t mk(const void* p, u32 bytes) { return __builtin_amdgcn_make_buffer_rsrc((void*)p, 0, (int)bytes, 0x00020000); }
DI float bflo(u32 v) { return __uint_as_float(v << 16); }
DI float bfhi(u32 v) { return __uint_as_float(v & 0xffff0000u); }
DI f32x2 unp(u32 v) { f32x2 r = {bflo(v), bfhi(v)}; return r; }
DI u32 pkv(f32x2 v) { return __builtin_bit_cast(u32, __builtin_convertvector(v, bf16x2)); }
DI u32 pk2(float a, float b) { f32x2 v = {a, b}; return pkv(v); }
DI float rbf(float a) { return bflo(pk2(a, a)); }
DI f32x2 bc(float a) { f32x2 r = {a, a}; return r; }
DI f32x2 fma2(f32x2 a, f32x2 b, f32x2 c) { return __builtin_elementwise_fma(a, b, c); }
DI f32x2 lo2(f32x4 v) { f32x2 r = {v[0], v[1]}; return r; }
DI f32x2 hi2(f32x4 v) { f32x2 r = {v[2], v[3]}; return r; }
template <int R> DI f32x2 pr(const f32x16& a) { f32x2 r = {a[R], a[R + 1]}; return r; }
DI float dot2(u32 a, u32 b, float c) { return __builtin_amdgcn_fdot2_f32_bf16(__builtin_bit_cast(bf16x2, a), __builtin_bit_cast(bf16x2, b), c, false); }
DI bf16x8 asb(v4u v) { return __builtin_bit_cast(bf16x8, v); }
DI f32x16 mfma(v4u a, v4u b, f32x16 c) { return __builtin_amdgcn_mfma_f32_32x32x16_bf16(asb(a), asb(b), c, 0, 0, 0); }
DI float half_sum(float v) { auto s = __builtin_amdgcn_permlane32_swap(__float_as_uint(v), __float_as_uint(v), false, false); return __uint_as_float(s[0]) + __uint_as_float(s[1]); }
DI float half_max(float v) { auto s = __builtin_amdgcn_permlane32_swap(__float_as_uint(v), __float_as_uint(v), false, false); return fmaxf(__uint_as_float(s[0]), __uint_as_float(s[1])); }
DI float half_min(float v) { auto s = __builtin_amdgcn_permlane32_swap(__float_as_uint(v), __float_as_uint(v), false, false); return fminf(__uint_as_float(s[0]), __uint_as_float(s[1])); }
DI float half_hi(float v) { auto s = __builtin_amdgcn_permlane32_swap(__float_as_uint(v), __float_as_uint(v), false, false); return __uint_as_float(s[1]); }
template <int CTRL> DI float dpp_add(float v) { return v + __int_as_float(__builtin_amdgcn_update_dpp(0, __float_as_int(v), CTRL, 0xf, 0xf, true)); }
#define ROR2 0x122
#define ROR4 0x124
#define ROR8 0x128
#define BAR() asm volatile("s_waitcnt lgkmcnt(0)\n\ts_barrier" ::: "memory")
#define VMW(n) asm volatile("s_waitcnt vmcnt(%0)" ::"n"(n) : "memory")
#define SCHED() __builtin_amdgcn_sched_barrier(0)

template <int N, int L0, int LS, int G0, int GS> DI void dma(u32 voff, rsrc_t rs, u32 sg, u32 sl) {
  u32 t;
  asm volatile("s_add_u32 m0, %3, %5\n\ts_add_u32 %0, %2, %6\n\tbuffer_load_dwordx4 %1, %4, %0 offen lds\n\t"
               ".rept %7\n\ts_add_u32 m0, m0, %8\n\ts_add_u32 %0, %0, %9\n\tbuffer_load_dwordx4 %1, %4, %0 offen lds\n\t.endr"
               : "=&s"(t) : "v"(voff), "s"(sg), "s"(sl), "s"(rs), "n"(L0), "n"(G0), "n"(N - 1), "n"(LS), "n"(GS) : "memory", "scc");
}
template <int R> DI void dma_x4(u32 voff, rsrc_t rs, u32 sg, u32 sl) {
  u32 t;
  asm volatile("s_add_u32 m0, %3, %5\n\ts_add_u32 %0, %2, %5\n\tbuffer_load_dwordx4 %1, %4, %0 offen lds\n\t"
               "s_add_u32 m0, m0, 896\n\ts_add_u32 %0, %0, 896\n\tbuffer_load_dwordx4 %1, %4, %0 offen lds\n\t"
               "s_add_u32 m0, m0, 13440\n\ts_add_u32 %0, %0, 13440\n\tbuffer_load_dwordx4 %1, %4, %0 offen lds\n\t"
               "s_add_u32 m0, m0, 896\n\ts_add_u32 %0, %0, 896\n\tbuffer_load_dwordx4 %1, %4, %0 offen lds"
               : "=&s"(t) : "v"(voff), "s"(sg), "s"(sl), "s"(rs), "n"(R * 896) : "memory", "scc");
}
template <int L0, int G0> DI void dma3(u32 v0, u32 v1, u32 v2, rsrc_t rs, u32 sg, u32 sl) {
  u32 t;
  asm volatile("s_add_u32 m0, %5, %7\n\ts_add_u32 %0, %4, %8\n\tbuffer_load_dwordx4 %1, %6, %0 offen lds\n\t"
               "s_add_u32 m0, m0, 2048\n\ts_nop 0\n\tbuffer_load_dwordx4 %2, %6, %0 offen lds\n\t"
               "s_add_u32 m0, m0, 2048\n\ts_nop 0\n\tbuffer_load_dwordx4 %3, %6, %0 offen lds"
               : "=&s"(t) : "v"(v0), "v"(v1), "v"(v2), "s"(sg), "s"(sl), "s"(rs), "n"(L0), "n"(G0) : "memory", "scc");
}
template <int IMM> DI void ald128(v4u& d, u32 voff, rsrc_t rs, u32 soff) {
  asm volatile("buffer_load_dwordx4 %0, %1, %2, %3 offen offset:%4" : "=v"(d) : "v"(voff), "s"(rs), "s"(soff), "n"(IMM) : "memory");
}
template <int IMM> DI void ald128c(v4u& d, u32 voff, rsrc_t rs, u32 soff) {
  asm volatile("buffer_load_dwordx4 %0, %1, %2, %3 offen offset:%4 sc1" : "=v"(d) : "v"(voff), "s"(rs), "s"(soff), "n"(IMM) : "memory");
}
DI void ald64(v2u& d, u32 voff, rsrc_t rs, u32 soff) { asm volatile("buffer_load_dwordx2 %0, %1, %2, %3 offen" : "=v"(d) : "v"(voff), "s"(rs), "s"(soff) : "memory"); }
DI void ald64c(v2u& d, u32 voff, rsrc_t rs, u32 soff) { asm volatile("buffer_load_dwordx2 %0, %1, %2, %3 offen sc1" : "=v"(d) : "v"(voff), "s"(rs), "s"(soff) : "memory"); }
#define TIE(x) asm volatile("" : "+v"(x))

DI v4u ld128(rsrc_t r, u32 off, u32 so = 0) { return __builtin_amdgcn_raw_buffer_load_b128(r, off, so, 0); }
DI v4u ld128c(rsrc_t r, u32 off, u32 so = 0) { return __builtin_amdgcn_raw_buffer_load_b128(r, off, so, 16); }
DI v2u ld64(rsrc_t r, u32 off, u32 so = 0) { return __builtin_amdgcn_raw_buffer_load_b64(r, off, so, 0); }
DI v2u ld64c(rsrc_t r, u32 off, u32 so = 0) { return __builtin_amdgcn_raw_buffer_load_b64(r, off, so, 16); }
DI void st128c(rsrc_t r, u32 off, u32 so, v4u v) { __builtin_amdgcn_raw_buffer_store_b128(v, r, off, so, 16); }
DI void st64c(rsrc_t r, u32 off, u32 so, v2u v) { __builtin_amdgcn_raw_buffer_store_b64(v, r, off, so, 16); }
DI void st32c(rsrc_t r, u32 off, u32 so, u32 v) { __builtin_amdgcn_raw_buffer_store_b32(v, r, off, so, 16); }

struct Lds {
  LAS unsigned char* p;
  DI v4u r128(u32 o) const { return *(LAS v4u*)(p + o); }
  DI v2u r64(u32 o) const { return *(LAS v2u*)(p + o); }
  DI f32x4 rf4(u32 o) const { return *(LAS f32x4*)(p + o); }
  DI f32x2 rf2(u32 o) const { return *(LAS f32x2*)(p + o); }
  DI float rf(u32 o) const { return *(LAS float*)(p + o); }
  DI void w128(u32 o, v4u v) const { *(LAS v4u*)(p + o) = v; }
  DI void w64(u32 o, v2u v) const { *(LAS v2u*)(p + o) = v; }
  DI void wf4(u32 o, f32x4 v) const { *(LAS f32x4*)(p + o) = v; }
  DI void wf2(u32 o, f32x2 v) const { *(LAS f32x2*)(p + o) = v; }
  DI void wf(u32 o, float v) const { *(LAS float*)(p + o) = v; }
  DI v2u tr(u32 o) const { return __builtin_bit_cast(v2u, __builtin_amdgcn_ds_read_tr16_b64_v4i16((LAS s16x4*)(p + o))); }
  DI v4u tr2(u32 o0, u32 o1) const { v2u a = tr(o0), b = tr(o1); v4u r = {a.x, a.y, b.x, b.y}; return r; }
};

DI void wait_flags(rsrc_t ws, u32 off, bool active, u32 tlo, u32 thi, u32 code) {
  for (int spins = 0;; ++spins) {
    asm volatile("" ::: "memory");
    v2u v = ld64c(ws, off);
    bool ok = !active || (v.x == tlo && v.y == thi);
    if (__all(ok)) break;
    if (spins > SPIN_CAP) { st32c(ws, WS_TMO, 0, code); break; }
    __builtin_amdgcn_s_sleep(1);
  }
  asm volatile("" ::: "memory");
}
DI void set_flag(rsrc_t ws, u32 idx, u32 tlo, u32 thi) { v2u t = {tlo, thi}; st64c(ws, WS_FLAG + idx * 256, 0, t); }
DI bool gran_ok(v4u a, u32 glo, u32 ghi) { return (a.x & 0xffff0000u) == glo && a.y == ghi && (a.z & 0xffff0000u) == glo && a.w == ghi; }

#define STAMP(i)

DI void feature_map(const f32x16& pin, int h, u32 (&P1)[8], u32 (&P2)[8]) {
  const float L2E = 1.4426950408889634f, INF = __builtin_inff();
  float pm[16], pn[16];
#pragma unroll
  for (int r = 0; r < 16; ++r) { pm[r] = pin[r]; pn[r] = pin[r]; }
#pragma unroll
  for (int r = 12; r < 16; ++r) { pm[r] = h ? -INF : pin[r]; pn[r] = h ? INF : pin[r]; }
  float mx = fmaxf(pm[0], pm[1]), mn = fminf(pn[0], pn[1]);
#pragma unroll
  for (int r = 2; r < 16; r += 2) { mx = fmaxf(fmaxf(mx, pm[r]), pm[r + 1]); mn = fminf(fminf(mn, pn[r]), pn[r + 1]); }
  mx = half_max(mx); mn = half_min(mn);
  const f32x2 cmx = bc(-mx * L2E), cmn = bc(mn * L2E), pl = bc(L2E), nl = bc(-L2E);
  f32x2 e1[8], e2[8];
#pragma unroll
  for (int i = 0; i < 8; ++i) {
    const f32x2 a = {pm[2 * i], pm[2 * i + 1]}, b = {pn[2 * i], pn[2 * i + 1]};
    const f32x2 ta = fma2(a, pl, cmx), tb = fma2(b, nl, cmn);
    e1[i] = f32x2{__builtin_amdgcn_exp2f(ta.x), __builtin_amdgcn_exp2f(ta.y)};
    e2[i] = f32x2{__builtin_amdgcn_exp2f(tb.x), __builtin_amdgcn_exp2f(tb.y)};
  }
  f32x2 s1 = ((e1[0] + e1[1]) + (e1[2] + e1[3])) + ((e1[4] + e1[5]) + (e1[6] + e1[7]));
  f32x2 s2 = ((e2[0] + e2[1]) + (e2[2] + e2[3])) + ((e2[4] + e2[5]) + (e2[6] + e2[7]));
  const f32x2 r1 = bc(__builtin_amdgcn_rcpf(half_sum(s1.x + s1.y))), r2 = bc(__builtin_amdgcn_rcpf(half_sum(s2.x + s2.y)));
#pragma unroll
  for (int i = 0; i < 8; ++i) { P1[i] = pkv(e1[i] * r1); P2[i] = pkv(e2[i] * r2); }
}

template <int NV> DI void gemv_reduce_store(const Lds& L, u32 scratch, f32x2 (&acc)[NV][4], int w, int lane) {
  float r[NV][8];
#pragma unroll
  for (int v = 0; v < NV; ++v)
#pragma unroll
    for (int j = 0; j < 8; ++j) { float a = acc[v][j >> 1][j & 1]; a = dpp_add<ROR2>(a); a = dpp_add<ROR4>(a); r[v][j] = dpp_add<ROR8>(a); }
  if ((lane & 14) == 0) {
    const u32 base = scratch + (w * 4 + (lane >> 4)) * (NV * 64) + (lane & 1) * 32;
#pragma unroll
    for (int v = 0; v < NV; ++v) {
      f32x4 a = {r[v][0], r[v][1], r[v][2], r[v][3]}, b = {r[v][4], r[v][5], r[v][6], r[v][7]};
      L.wf4(base + v * 64, a); L.wf4(base + v * 64 + 16, b);
    }
  }
}
DI float silu_bf(float c) { return rbf(c * __builtin_amdgcn_rcpf(1.f + __builtin_amdgcn_exp2f(-1.4426950408889634f * c))); }

DI v4u scale_row(v4u wv, f32x2 gs, f32x2 (&cs)[4], f32x2 (&bs)[4]) {
  v4u o;
  const f32x2 g = bc(gs.x), s = bc(gs.y);
#pragma unroll
  for (int e = 0; e < 4; ++e) {
    const f32x2 a = unp(wv[e]);
    const f32x2 m = a * g;
    o[e] = pkv(m);
    cs[e] += m;
    bs[e] = fma2(a, s, bs[e]);
  }
  return o;
}

__global__ void __launch_bounds__(256, 2)
fused_block(const u16* __restrict__ gx, const u16* __restrict__ gwqkv, const u16* __restrict__ gwphi, const u16* __restrict__ gwo,
            const u16* __restrict__ gwg, const u16* __restrict__ gwu, const u16* __restrict__ gwd, const u16* __restrict__ gc,
            const u16* __restrict__ gwmod, u16* __restrict__ gout, unsigned char* __restrict__ gws) {
  extern __shared__ __attribute__((aligned(16))) unsigned char smem[];
  asm volatile("" ::"s"(gx), "s"(gwqkv), "s"(gwphi), "s"(gwo), "s"(gwg), "s"(gwu), "s"(gwd), "s"(gc), "s"(gwmod), "s"(gout), "s"(gws));
  Lds L; L.p = (LAS unsigned char*)smem;
  const u32 lds0 = (u32)(uintptr_t)L.p;
  int tid = threadIdx.x, lane = tid & 63;
  const int w = __builtin_amdgcn_readfirstlane(tid >> 6);
  const int bid = blockIdx.x, xq = bid & 7, iq = bid >> 3;
  int h, t32, g16, q4, p4;
#define RELANE() do { asm volatile("" : "+v"(tid)); lane = tid & 63; h = lane >> 5; t32 = lane & 31; g16 = (lane >> 4) & 1; q4 = (lane & 15) >> 2; p4 = lane & 3; } while (0)
  RELANE();

  const rsrc_t rWS = mk(gws, WS_USED);
  const rsrc_t rX = mk(gx, 2048 * 896), rWQKV = mk(gwqkv, 448 * 2688), rWPHI = mk(gwphi, 16 * 784 * 2), rWO = mk(gwo, 448 * 896);
  const rsrc_t rWG = mk(gwg, 448 * 3584), rWU = mk(gwu, 448 * 3584), rWD = mk(gwd, 1792 * 896), rC = mk(gc, 896), rWM = mk(gwmod, 448 * 5376);
  const rsrc_t rOUT = mk(gout, 2048 * 896);

  const int hd = 8 * (xq & 1) + (iq & 7), tt = 4 * (xq >> 1) + (iq >> 3);
  const bool p24 = iq < 28;
  const int t64 = 4 * xq + iq / 7, cs = iq % 7;
  const int chunk = (iq >> 2) * 32 + xq * 4 + (iq & 3);
  const bool p0 = chunk < 168 && iq < 24, crit = iq < 8, p0c = iq < 28;
  const u32 zero = 0;

  STAMP(0);
  v2u ep;
  ald64c(ep, zero, rWS, zero);
  v2u cw; v4u wm[4];
  const u32 vgemv = (tid >> 1) * 4;
  auto p0_loads = [&]() {
    ald64(cw, vgemv * 2, rC, zero);
    const u32 vo = vgemv * 5376 + chunk * 32 + (tid & 1) * 16;
    sfor<0, 4>([&](auto j) { ald128<0>(wm[j.value], vo, rWM, j.value * 5376); });
  };
  if (crit) p0_loads();
  if (lane < 56) {
    const u32 sg = (tt * 128 + w * 32) * 896, sl = lds0 + L_XS + w * 28672;
    sfor<0, 8>([&](auto k) { dma_x4<2 * k.value>((lane * 16) ^ (k.value * 16), rX, sg, sl); });
  }
  v4u wkv[14];
  {
    const u32 vo = (tid >> 3) * 2688 + (1 + ((tid >> 2) & 1)) * 896 + hd * 56 + (tid & 3) * 16;
    sfor<0, 14>([&](auto i) { ald128<0>(wkv[i.value], vo, rWQKV, i.value * 32 * 2688); });
  }
  v2u wph;
  ald64(wph, ((tid >> 3) < 28 && (tid & 7) < 7) ? (hd * 784 + (tid >> 3) * 28 + (tid & 7) * 4) * 2 : 0x7ffffff0, rWPHI, zero);
  if (p0 && !crit) p0_loads();

  u32 tlo, thi, glo, ghi;
  auto get_tag = [&]() {
    const u64 E = ((u64)__builtin_amdgcn_readfirstlane(ep.y) << 32) | (u32)__builtin_amdgcn_readfirstlane(ep.x);
    const u64 tag = E + 1;
    tlo = (u32)tag; thi = (u32)(tag >> 32);
    glo = tlo << 16; ghi = (u32)(tag >> 16);
  };
  auto p0_math = [&]() {
    f32x2 acc[1][4] = {};
    const float sv[4] = {silu_bf(bflo(cw.x)), silu_bf(bfhi(cw.x)), silu_bf(bflo(cw.y)), silu_bf(bfhi(cw.y))};
#pragma unroll
    for (int j = 0; j < 4; ++j)
#pragma unroll
      for (int e = 0; e < 4; ++e) acc[0][e] = fma2(unp(wm[j][e]), bc(sv[j]), acc[0][e]);
    gemv_reduce_store<1>(L, L_P0S, acc, w, lane);
    BAR();
    if (w == 0) {
      const int rep = lane >> 3, pr_ = lane & 7;
      f32x2 v = {0.f, 0.f};
#pragma unroll
      for (int gI = 0; gI < 16; ++gI) v += L.rf2(L_P0S + gI * 64 + pr_ * 8);
      float v0 = rbf(v.x), v1 = rbf(v.y);
      if ((chunk >= 28 && chunk < 56) || (chunk >= 112 && chunk < 140)) { v0 = 1.f + v0; v1 = 1.f + v1; }
      const u32 pk = pk2(v0, v1);
      v4u gr = {glo | (pk & 0xffffu), ghi, glo | (pk >> 16), ghi};
      st128c(rWS, rep * MODG_REP + 2 * pr_ * 8, WS_MODG + chunk * 128, gr);
    }
  };
  if (crit) {
    asm volatile("s_waitcnt vmcnt(47)" : "+v"(ep), "+v"(cw), "+v"(wm[0]), "+v"(wm[1]), "+v"(wm[2]), "+v"(wm[3])::"memory");
    get_tag();
    p0_math();
  }
  STAMP(1);

  if (crit) { if (w == 0) VMW(16); else VMW(15); } else if (p0) VMW(20); else VMW(15);
  v4u X[28];
  {
    const u32 gsw = (t32 >> 1) & 7;
    u32 xb[4];
#pragma unroll
    for (int j = 0; j < 4; ++j) xb[j] = L_XS + w * 28672 + t32 * 896 + (((2 * j + h) ^ gsw) * 16);
#pragma unroll
    for (int s = 0; s < 28; ++s) X[s] = L.r128(xb[s & 3] + (s >> 2) * 128);
  }
  float rstd1, mr1;
  {
    float s0 = 0.f, s1 = 0.f, q0 = 0.f, q1 = 0.f;
#pragma unroll
    for (int s = 0; s < 28; ++s) {
      s0 = dot2(X[s][0], 0x3f803f80u, s0); s1 = dot2(X[s][1], 0x3f803f80u, s1);
      s0 = dot2(X[s][2], 0x3f803f80u, s0); s1 = dot2(X[s][3], 0x3f803f80u, s1);
      q0 = dot2(X[s][0], X[s][0], q0); q1 = dot2(X[s][1], X[s][1], q1);
      q0 = dot2(X[s][2], X[s][2], q0); q1 = dot2(X[s][3], X[s][3], q1);
    }
    const float m = half_sum(s0 + s1) * (1.f / 448.f);
    const float var = fmaxf(half_sum(q0 + q1) * (1.f / 448.f) - m * m, 0.f);
    rstd1 = __builtin_amdgcn_rsqf(var + 1e-5f);
    mr1 = -m * rstd1;
    if (h == 0) { f32x2 st = {rstd1, mr1}; L.wf2(L_ST1 + (w * 32 + t32) * 8, st); }
  }
  asm volatile("s_waitcnt vmcnt(0)" : "+v"(ep), "+v"(wph), "+v"(wkv[0]), "+v"(wkv[1]), "+v"(wkv[2]), "+v"(wkv[3]), "+v"(wkv[4]), "+v"(wkv[5]), "+v"(wkv[6])::"memory");
  asm volatile("" : "+v"(wkv[7]), "+v"(wkv[8]), "+v"(wkv[9]), "+v"(wkv[10]), "+v"(wkv[11]), "+v"(wkv[12]), "+v"(wkv[13]));
  L.w64(L_WPH + (tid >> 3) * 64 + (tid & 7) * 8, wph);
  if (!crit) {
    get_tag();
    if (p0) { asm volatile("" : "+v"(cw), "+v"(wm[0]), "+v"(wm[1]), "+v"(wm[2]), "+v"(wm[3])); p0_math(); }
  }
  STAMP(2);

  RELANE();
  {
    const u32 so = WS_MODG + xq * MODG_REP;
    const bool act = tid < 224;
    v4u a, b;
    for (int spins = 0;; ++spins) {
      asm volatile("" ::: "memory");
      a = ld128c(rWS, tid * 16, so); b = ld128c(rWS, tid * 16 + 448 * 8, so);
      const bool ok = !act || (gran_ok(a, glo, ghi) && gran_ok(b, glo, ghi));
      if (__all(ok)) break;
      if (spins > SPIN_CAP) { st32c(rWS, WS_TMO, 0, 1); break; }
      __builtin_amdgcn_s_sleep(2);
    }
    if (act) { f32x4 v = {bflo(b.x), bflo(a.x), bflo(b.z), bflo(a.z)}; L.wf4(L_T1 + tid * 16, v); }
  }
  BAR();
  STAMP(3);

  v4u wq[7];
  {
    f32x2 cs8[4] = {}, bs8[4] = {};
    const u32 r0 = tid >> 3;
    const u32 wb = L_WKV + r0 * 128 + (((tid & 7) ^ (((r0 >> 1) & 1) << 2)) * 16);
#pragma unroll
    for (int i = 0; i < 14; ++i) L.w128(wb + i * 4096, scale_row(wkv[i], L.rf2(L_T1 + r0 * 8 + i * 256), cs8, bs8));
    {
      const u32 vo = (tid >> 2) * 2688 + hd * 56 + (tid & 3) * 16;
      sfor<0, 7>([&](auto i) { ald128<0>(wq[i.value], vo, rWQKV, i.value * 64 * 2688); });
    }
    float c8[8], b8[8];
#pragma unroll
    for (int j = 0; j < 8; ++j) { c8[j] = dpp_add<ROR8>(cs8[j >> 1][j & 1]); b8[j] = dpp_add<ROR8>(bs8[j >> 1][j & 1]); }
    if ((lane & 8) == 0) {
      const u32 base = L_CSP + (w * 4 + (lane >> 4)) * 512 + (lane & 7) * 32;
      f32x4 a = {c8[0], c8[1], c8[2], c8[3]}, b = {c8[4], c8[5], c8[6], c8[7]};
      f32x4 c = {b8[0], b8[1], b8[2], b8[3]}, d = {b8[4], b8[5], b8[6], b8[7]};
      L.wf4(base, a); L.wf4(base + 16, b); L.wf4(base + 256, c); L.wf4(base + 272, d);
    }
  }
  BAR();
  STAMP(4);

  f32x16 accK = {}, accV = {};
  {
    float csum = 0.f;
    const u32 cso = L_CSP + (w >> 1) * 256 + ((w & 1) * 32 + t32) * 4 + h * 8 * 512;
    const u32 sw = ((q4 >> 1) & 1) << 2;
    const u32 rowb = L_WKV + (8 * h + q4) * 128 + (p4 & 1) * 8;
    const u32 bk = rowb + (((2 * g16 + (p4 >> 1)) ^ sw) * 16), bv = rowb + (((4 + 2 * g16 + (p4 >> 1)) ^ sw) * 16);
    v4u fk[2], fv[2];
    fk[0] = L.tr2(bk, bk + 512); fv[0] = L.tr2(bv, bv + 512);
#pragma unroll
    for (int s = 0; s < 28; ++s) {
      accK = mfma(fk[s & 1], X[s], accK);
      if (s + 1 < 28) fk[(s + 1) & 1] = L.tr2(bk + (s + 1) * 2048, bk + (s + 1) * 2048 + 512);
      if (s < 8) csum += L.rf(cso + s * 512);
      SCHED();
      accV = mfma(X[s], fv[s & 1], accV);
      if (s + 1 < 28) fv[(s + 1) & 1] = L.tr2(bv + (s + 1) * 2048, bv + (s + 1) * 2048 + 512);
      if (s == 9) { csum = half_sum(csum); if (h == 0) L.wf(L_CSF + (w >> 1) * 384 + ((w & 1) * 32 + t32) * 4, csum); }
      SCHED();
    }
  }
  asm volatile("s_waitcnt vmcnt(0)" : "+v"(wq[0]), "+v"(wq[1]), "+v"(wq[2]), "+v"(wq[3]), "+v"(wq[4]), "+v"(wq[5]), "+v"(wq[6])::"memory");
  BAR();
  STAMP(5);
  v4u PH[2];
  {
    const u32 b = L_WPH + (4 * h + q4) * 64 + g16 * 32 + p4 * 8;
    PH[0] = L.tr2(b, b + 8 * 64); PH[1] = L.tr2(b + 16 * 64, b + 24 * 64);
  }
  auto ln_epi_t = [&](const f32x16& a, u32 col0, v4u& b0, v4u& b1) {
    u32 o[8];
    sfor<0, 4>([&](auto qq) {
      constexpr int Q = qq.value;
      const f32x4 c4 = L.rf4(L_CSF + (col0 + 8 * Q + 4 * h) * 4), b4 = L.rf4(L_CSF + 384 + (col0 + 8 * Q + 4 * h) * 4);
      o[2 * Q] = pkv(fma2(pr<4 * Q>(a), bc(rstd1), fma2(lo2(c4), bc(mr1), lo2(b4))));
      o[2 * Q + 1] = pkv(fma2(pr<4 * Q + 2>(a), bc(rstd1), fma2(hi2(c4), bc(mr1), hi2(b4))));
    });
    b0 = v4u{o[0], o[1], o[2], o[3]}; b1 = v4u{o[4], o[5], o[6], o[7]};
  };
  {
    v4u b0, b1;
    ln_epi_t(accK, 0, b0, b1);
    f32x16 pk = {};
    pk = mfma(PH[0], b0, pk); pk = mfma(PH[1], b1, pk);
    {
      const float cv_ = L.rf(L_CSF + (32 + t32) * 4), bv_ = L.rf(L_CSF + 384 + (32 + t32) * 4);
      const float keep = t32 < 28 ? 1.f : 0.f, one = t32 == 28 ? 1.f : 0.f;
      u32 vb[8];
#pragma unroll
      for (int qq = 0; qq < 4; ++qq) {
        const u32 so = L_ST1 + (w * 32 + 8 * qq + 4 * h) * 8;
        const f32x4 s01 = L.rf4(so), s23 = L.rf4(so + 16);
        float v[4];
        v[0] = fmaf(accV[4 * qq + 0], s01[0], fmaf(s01[1], cv_, bv_));
        v[1] = fmaf(accV[4 * qq + 1], s01[2], fmaf(s01[3], cv_, bv_));
        v[2] = fmaf(accV[4 * qq + 2], s23[0], fmaf(s23[1], cv_, bv_));
        v[3] = fmaf(accV[4 * qq + 3], s23[2], fmaf(s23[3], cv_, bv_));
#pragma unroll
        for (int e = 0; e < 4; ++e) v[e] = t32 < 28 ? v[e] : one;
        vb[2 * qq] = pk2(v[0], v[1]); vb[2 * qq + 1] = pk2(v[2], v[3]);
      }
      (void)keep;
      v4u a = {vb[0], vb[1], vb[2], vb[3]}, b = {vb[4], vb[5], vb[6], vb[7]};
      L.w128(L_VFR + w * 2048 + lane * 16, a); L.w128(L_VFR + w * 2048 + 1024 + lane * 16, b);
    }
    u32 P1[8], P2[8];
    feature_map(pk, h, P1, P2);
    const u32 swt = (t32 & 7) | ((((t32 >> 3) ^ (t32 >> 1)) & 1) << 3);
    const u32 rb = L_PHI + w * 4096 + t32 * 128;
#pragma unroll
    for (int qq = 0; qq < 4; ++qq) {
      v2u a = {P1[2 * qq], P1[2 * qq + 1]}, b = {P2[2 * qq], P2[2 * qq + 1]};
      L.w64(rb + (((2 * qq + h) ^ swt) * 8), a);
      L.w64(rb + (((8 + 2 * qq + h) ^ swt) * 8), b);
    }
  }
  BAR();
  STAMP(6);
  if (w < 2) {
    f32x16 kv = {};
    u32 ad[4];
#pragma unroll
    for (int su = 0; su < 4; ++su) {
      const u32 t = 8 * su + 4 * h + q4;
      const u32 sw_ = (t & 7) | ((((t >> 3) ^ (t >> 1)) & 1) << 3);
      ad[su] = L_PHI + t * 128 + (((8 * w + 4 * g16 + p4) ^ sw_) * 8);
    }
#pragma unroll
    for (int ww = 0; ww < 4; ++ww)
#pragma unroll
      for (int s = 0; s < 2; ++s) {
        const v4u a = L.tr2(ad[2 * s] + ww * 4096, ad[2 * s + 1] + ww * 4096);
        const v4u b = L.r128(L_VFR + ww * 2048 + s * 1024 + lane * 16);
        kv = mfma(a, b, kv);
      }
    v4u o0 = {pkv(pr<0>(kv)), pkv(pr<2>(kv)), pkv(pr<4>(kv)), pkv(pr<6>(kv))};
    v4u o1 = {pkv(pr<8>(kv)), pkv(pr<10>(kv)), pkv(pr<12>(kv)), pkv(pr<14>(kv))};
    const u32 so = WS_KVP + (hd * 16 + tt) * 4096 + w * 2048;
    st128c(rWS, lane * 16, so, o0); st128c(rWS, lane * 16 + 1024, so, o1);
  }
  STAMP(7);
  RELANE();
  const u32 sl_w = lds0 + w * 1024;
  {
    const u32 rr = lane >> 3, c = (lane & 7) ^ (((rr >> 1) & 1) << 2);
    const u32 voff = rr * 896 + cs * 128 + c * 16, sg = w * 7168;
    if (p24) dma<14, L_WO, 4096, 0, 28672>(voff, rWO, sg, sl_w);
  }
  {
    f32x2 cs8[4] = {}, bs8[4] = {};
    const u32 r0 = tid >> 2;
#pragma unroll
    for (int i = 0; i < 7; ++i) L.w128(L_WQ + r0 * 64 + (tid & 3) * 16 + i * 4096, scale_row(wq[i], L.rf2(L_T1 + r0 * 8 + i * 512), cs8, bs8));
    float c8[8], b8[8];
#pragma unroll
    for (int j = 0; j < 8; ++j) { c8[j] = dpp_add<ROR8>(dpp_add<ROR4>(cs8[j >> 1][j & 1])); b8[j] = dpp_add<ROR8>(dpp_add<ROR4>(bs8[j >> 1][j & 1])); }
    if ((lane & 12) == 0) {
      const u32 base = L_CSQ + (w * 4 + (lane >> 4)) * 256 + (lane & 3) * 32;
      f32x4 a = {c8[0], c8[1], c8[2], c8[3]}, b = {c8[4], c8[5], c8[6], c8[7]};
      f32x4 c = {b8[0], b8[1], b8[2], b8[3]}, d = {b8[4], b8[5], b8[6], b8[7]};
      L.wf4(base, a); L.wf4(base + 16, b); L.wf4(base + 128, c); L.wf4(base + 144, d);
    }
  }
  if (p24) VMW(14); else VMW(0);
  BAR();
  if (tid == 0) set_flag(rWS, FL_KV + hd * 16 + tt, tlo, thi);
  STAMP(8);
  f32x16 accQ = {};
  {
    float csum = 0.f;
    const u32 cso = L_CSQ + (w & 1) * 128 + t32 * 4 + h * 8 * 256;
    const u32 bq = L_WQ + (8 * h + q4) * 64 + g16 * 32 + p4 * 8;
    v4u fq[2];
    fq[0] = L.tr2(bq, bq + 256);
#pragma unroll
    for (int s = 0; s < 28; ++s) {
      accQ = mfma(fq[s & 1], X[s], accQ);
      if (s + 1 < 28) fq[(s + 1) & 1] = L.tr2(bq + (s + 1) * 1024, bq + (s + 1) * 1024 + 256);
      if (s < 8) csum += L.rf(cso + s * 256);
      if (s == 9) { csum = half_sum(csum); if (h == 0 && w < 2) L.wf(L_CSF + w * 384 + (64 + t32) * 4, csum); }
      SCHED();
    }
  }
  if (w == 0) wait_flags(rWS, WS_FLAG + (FL_KV + hd * 16 + (lane & 15)) * 256, lane < 16, tlo, thi, 3);
  BAR();
  STAMP(9);
  RELANE();
  v4u pp[16];
  {
    const u32 so = WS_KVP + hd * 16 * 4096 + w * 1024;
#pragma unroll
    for (int i = 0; i < 16; ++i) pp[i] = ld128c(rWS, lane * 16, so + i * 4096);
  }
  u32 Q1[8], Q2[8];
  {
    v4u b0, b1;
    ln_epi_t(accQ, 64, b0, b1);
    f32x16 pq = {};
    pq = mfma(PH[0], b0, pq); pq = mfma(PH[1], b1, pq);
    feature_map(pq, h, Q1, Q2);
  }
  STAMP(10);
  {
    f32x2 s[4] = {};
    float sv[8] = {};
    u32 selLo, selHi;
    asm("s_mov_b32 %0, 0x3f80" : "=s"(selLo));
    asm("s_mov_b32 %0, 0x3f800000" : "=s"(selHi));
#pragma unroll
    for (int i = 0; i < 16; ++i)
#pragma unroll
      for (int e = 0; e < 4; ++e) { sv[2 * e] = dot2(pp[i][e], selLo, sv[2 * e]); sv[2 * e + 1] = dot2(pp[i][e], selHi, sv[2 * e + 1]); }
    (void)s;
    v4u o = {pk2(sv[0], sv[1]), pk2(sv[2], sv[3]), pk2(sv[4], sv[5]), pk2(sv[6], sv[7])};
    L.w128(L_KVR + w * 1024 + lane * 16, o);
  }
  v4u wc[4], mg2[2], msb[2];
  v2u mtg;
  const u32 so_mod = WS_MODG + xq * MODG_REP;
  const u32 vtg = ((tid >> 6) == 0 ? 896 : ((tid >> 6) == 1 ? 1792 : 2240)) * 8 + (cs * 64 + lane) * 8;
  auto mod_rest_loads = [&]() {
    const u32 vr = (vgemv < 448 ? vgemv : 0) * 8;
    mg2[0] = ld128c(rWS, vr + 1792 * 8, so_mod); mg2[1] = ld128c(rWS, vr + 1792 * 8 + 16, so_mod);
    msb[0] = ld128c(rWS, vr + 1344 * 8, so_mod); msb[1] = ld128c(rWS, vr + 1344 * 8 + 16, so_mod);
    mtg = ld64c(rWS, vtg, so_mod);
  };
  if (p0c) {
    const rsrc_t rW = chunk < 112 ? rWG : rWU;
    const int col0 = 16 * (chunk < 112 ? chunk : chunk - 112);
    const u32 vo = vgemv * 3584 + col0 * 2 + (tid & 1) * 16;
#pragma unroll
    for (int j = 0; j < 4; ++j) wc[j] = ld128(rW, vo, j * 3584);
    mod_rest_loads();
  }
  BAR();
  STAMP(11);
  {
    f32x16 o = {};
    v4u b0 = {Q1[0], Q1[1], Q1[2], Q1[3]}, b1 = {Q1[4], Q1[5], Q1[6], Q1[7]}, b2 = {Q2[0], Q2[1], Q2[2], Q2[3]}, b3 = {Q2[4], Q2[5], Q2[6], Q2[7]};
    o = mfma(L.r128(L_KVR + lane * 16), b0, o);
    o = mfma(L.r128(L_KVR + 1024 + lane * 16), b1, o);
    o = mfma(L.r128(L_KVR + 2048 + lane * 16), b2, o);
    o = mfma(L.r128(L_KVR + 3072 + lane * 16), b3, o);
    const float den = half_hi(o[12]) + 1e-6f;
    const float rd = __builtin_amdgcn_rcpf(den), rd2 = h ? 0.f : rd;
    const f32x2 r1 = bc(rd), r2 = bc(rd2);
    v4u o0 = {pkv(pr<0>(o) * r1), pkv(pr<2>(o) * r1), pkv(pr<4>(o) * r1), pkv(pr<6>(o) * r1)};
    v4u o1 = {pkv(pr<8>(o) * r1), pkv(pr<10>(o) * r1), pkv(pr<12>(o) * r2), pkv(pr<14>(o) * r2)};
    const u32 so = WS_OFR + ((tt * 4 + w) * 32 + hd * 2) * 1024;
    st128c(rWS, lane * 16, so, o0); st128c(rWS, lane * 16 + 1024, so, o1);
  }
  STAMP(12);
  RELANE();
  const int fs = hd;
  const u32 rrA = lane >> 3, pA = (lane & 7) >> 1, eA = lane & 1;
  const u32 vAG = rrA * 3584 + fs * 224 + ((pA ^ ((rrA >> 1) & 1)) * 32) + eA * 16;
  const u32 vAU = rrA * 3584 + fs * 224 + ((pA ^ 2 ^ ((rrA >> 1) & 1)) * 32) + eA * 16;
  const u32 sgA = w * 28672;
  u32 vB[3];
#pragma unroll
  for (int j = 0; j < 3; ++j) { const u32 pI = 128 * j + 64 * (w & 1) + lane; vB[j] = (pI / 6) * 3584 + fs * 224 + 128 + (pI % 6) * 16; }
  const rsrc_t rWB = w < 2 ? rWG : rWU;
  auto dma_B = [&](u32 sg, u32 sl) {
    sfor<0, 3>([&](auto a) { dma3<a.value * 6144, a.value * 64 * 3584>(vB[0], vB[1], vB[2], rWB, sg, sl); });
    dma<1, 9 * 2048, 0, 3 * 64 * 3584, 0>(vB[0], rWB, sg, sl);
    if ((w & 1) == 0) dma<1, 10 * 2048, 0, 3 * 64 * 3584, 0>(vB[1], rWB, sg, sl);
  };
  dma<10, L_AU + 4 * 4096, 4096, 4 * 114688, 114688>(vAU, rWU, sgA, sl_w);
  dma_B(0, lds0 + (w < 2 ? L_B1G : L_B1U) + (w & 1) * 1024);
  if (p0c) {
    for (int spins = 0;; ++spins) {
      const bool ok = gran_ok(mg2[0], glo, ghi) && gran_ok(mg2[1], glo, ghi) && gran_ok(msb[0], glo, ghi) && gran_ok(msb[1], glo, ghi) &&
                      (tid >= 192 || ((mtg.x & 0xffff0000u) == glo && mtg.y == ghi));
      if (__all(ok)) break;
      if (spins > SPIN_CAP) { st32c(rWS, WS_TMO, 0, 2); break; }
      asm volatile("" ::: "memory");
      mod_rest_loads();
    }
    f32x2 acc[2][4] = {};
    const float g2v[4] = {bflo(mg2[0].x), bflo(mg2[0].z), bflo(mg2[1].x), bflo(mg2[1].z)};
    const float sbv[4] = {bflo(msb[0].x), bflo(msb[0].z), bflo(msb[1].x), bflo(msb[1].z)};
#pragma unroll
    for (int j = 0; j < 4; ++j)
#pragma unroll
      for (int e = 0; e < 4; ++e) {
        const f32x2 a = unp(wc[j][e]);
        acc[0][e] = fma2(a, bc(g2v[j]), acc[0][e]);
        acc[1][e] = fma2(a, bc(sbv[j]), acc[1][e]);
      }
    gemv_reduce_store<2>(L, L_P0C, acc, w, lane);
    if (tid < 192) L.wf(L_TGA + tid * 4, bflo(mtg.x));
  }
  if ((w & 1) == 0) VMW(21); else VMW(20);
  BAR();
  if (tid == 0) set_flag(rWS, FL_O + tt * 16 + hd, tlo, thi);
  if (p0c && w == 1 && lane < 32) {
    const int mat = chunk >= 112, col0 = 16 * (chunk - 112 * mat);
    float a = 0.f;
#pragma unroll
    for (int gI = 0; gI < 16; ++gI) a += L.rf(L_P0C + gI * 128 + lane * 4);
    st32c(rWS, (((lane >> 4) * 2) * 1792 + (lane & 15)) * 4, WS_CSB + (mat * 1792 + col0) * 4, __float_as_uint(a));
  }
  STAMP(13);

  RELANE();
  u32 x1p[8] = {};
  const int tt2 = w & 1, kh = w >> 1;
  if (p24) {
    v2u xr[4];
    const u32 T32 = t64 * 2 + tt2;
    {
      const u32 vo = t32 * 896 + 8 * h, so = T32 * 32 * 896 + (cs * 64 + kh * 32) * 2;
#pragma unroll
      for (int qq = 0; qq < 4; ++qq) xr[qq] = ld64(rX, vo + 16 * qq, so);
    }
    if (w == 0) wait_flags(rWS, WS_FLAG + (FL_O + (t64 >> 1) * 16 + (lane & 15)) * 256, lane < 16, tlo, thi, 4);
    BAR();
    if (bid == 0 && tid == 0) { v2u t = {tlo, thi}; st64c(rWS, WS_EPOCH, 0, t); }
    STAMP(15);
    v4u O[16];
    {
      const u32 so = WS_OFR + (T32 * 32 + kh * 16) * 1024;
#pragma unroll
      for (int i = 0; i < 16; ++i) O[i] = ld128c(rWS, lane * 16 + (i & 3) * 1024, so + (i >> 2) * 4096);
    }
    f32x16 acc[2] = {};
    {
      const u32 sw = ((q4 >> 1) & 1) << 2;
      const u32 cb0 = (((2 * g16 + (p4 >> 1)) ^ sw) * 16) + (p4 & 1) * 8, cb1 = (((4 + 2 * g16 + (p4 >> 1)) ^ sw) * 16) + (p4 & 1) * 8;
      const u32 rb = L_WO + kh * 8 * 28 * 128 + q4 * 128;
      const u32 bh0 = rb + 4 * h * 128 + cb0, bh1 = rb + 4 * h * 128 + cb1, bn0 = rb + cb0, bn1 = rb + cb1;
      auto rd = [&](int i, int ct) -> v4u {
        const int hh = i >> 1, s = i & 1;
        const u32 o0 = (28 * hh + 16 * s) * 128, o1 = o0 + 8 * 128;
        const u32 b = ct ? bh1 : bh0, bn = ct ? bn1 : bn0;
        return L.tr2(b + o0, (s ? bn : b) + o1);
      };
      v4u f[2][2];
      f[0][0] = rd(0, 0); f[0][1] = rd(0, 1);
#pragma unroll
      for (int i = 0; i < 16; ++i) {
        acc[0] = mfma(f[i & 1][0], O[i], acc[0]);
        if (i + 1 < 16) f[(i + 1) & 1][0] = rd(i + 1, 0);
        SCHED();
        acc[1] = mfma(f[i & 1][1], O[i], acc[1]);
        if (i + 1 < 16) f[(i + 1) & 1][1] = rd(i + 1, 1);
        SCHED();
      }
    }
    {
      const f32x16 snd = kh ? acc[0] : acc[1];
      const u32 xb = L_XCH + w * 4096 + lane * 16;
#pragma unroll
      for (int qq = 0; qq < 4; ++qq) { f32x4 v = {snd[4 * qq], snd[4 * qq + 1], snd[4 * qq + 2], snd[4 * qq + 3]}; L.wf4(xb + qq * 1024, v); }
    }
    BAR();
    {
      const f32x16 mine = kh ? acc[1] : acc[0];
      const u32 xb = L_XCH + (w ^ 2) * 4096 + lane * 16;
      f32x2 x1[8];
      u32 a2[8];
      f32x2 sum = {0.f, 0.f};
      sfor<0, 4>([&](auto qq) {
        constexpr int Q = qq.value;
        const f32x4 o = L.rf4(xb + Q * 1024);
        const f32x4 ga4 = L.rf4(L_TGA + (kh * 32 + 8 * Q + 4 * h) * 4), g24 = L.rf4(L_TG2 + (kh * 32 + 8 * Q + 4 * h) * 4);
        x1p[2 * Q] = pkv(fma2(lo2(ga4), pr<4 * Q>(mine) + lo2(o), unp(xr[Q].x)));
        x1p[2 * Q + 1] = pkv(fma2(hi2(ga4), pr<4 * Q + 2>(mine) + hi2(o), unp(xr[Q].y)));
        x1[2 * Q] = unp(x1p[2 * Q]); x1[2 * Q + 1] = unp(x1p[2 * Q + 1]);
        a2[2 * Q] = pkv(x1[2 * Q] * lo2(g24)); a2[2 * Q + 1] = pkv(x1[2 * Q + 1] * hi2(g24));
        sum += x1[2 * Q] + x1[2 * Q + 1];
      });
      v4u o0 = {a2[0], a2[1], a2[2], a2[3]}, o1 = {a2[4], a2[5], a2[6], a2[7]};
      const u32 so = WS_X1F + (T32 * 28 + cs * 4 + kh * 2) * 1024;
      st128c(rWS, lane * 16, so, o0); st128c(rWS, lane * 16 + 1024, so, o1);
      const float mean = half_sum(sum.x + sum.y) * (1.f / 32.f);
      f32x2 m2v = {0.f, 0.f};
#pragma unroll
      for (int r = 0; r < 8; ++r) { const f32x2 d = x1[r] - bc(mean); m2v = fma2(d, d, m2v); }
      const float m2 = half_sum(m2v.x + m2v.y);
      if (h == 0) { v2u st = {__float_as_uint(mean), __float_as_uint(m2)}; st64c(rWS, t32 * 8, WS_STAT + (T32 * 14 + cs * 2 + kh) * 256, st); }
    }
  } else {
    BAR();
  }
  dma<14, L_AG, 4096, 0, 114688>(vAG, rWG, sgA, sl_w);
  VMW(14);
  BAR();
  if (p24 && tid == 0) set_flag(rWS, FL_X + t64 * 7 + cs, tlo, thi);
  STAMP(16);
  dma<4, L_AU, 4096, 0, 114688>(vAU, rWU, sgA, sl_w);
  if (w == 0) {
    u32 fo;
    if (lane < 14) fo = (tt * 2 + lane / 7) * 7 + lane % 7;
    else {
      const u32 c2 = lane < 21 ? 7 * fs + (lane - 14) : 112 + 7 * fs + (lane - 21);
      const u32 oiq = (c2 >> 5) * 4 + (c2 & 3), oxq = (c2 >> 2) & 7;
      fo = (4 * oxq + oiq / 7) * 7 + oiq % 7;
    }
    wait_flags(rWS, WS_FLAG + (FL_X + fo) * 256, lane < 28, tlo, thi, 5);
  }
  VMW(0);
  BAR();
  STAMP(17);
  RELANE();
  v4u B[28];
  const u32 T32p = tt * 4 + w;
  v2u st[14];
  v4u tcs;
  {
    const int kind = (tid / 28) & 3, j = tid % 28;
    tcs = ld128c(rWS, (kind * 1792 + j * 4) * 4, WS_CSB + fs * 448);
    const u32 so = WS_STAT + T32p * 14 * 256;
#pragma unroll
    for (int i = 0; i < 14; ++i) st[i] = ld64c(rWS, t32 * 8 + i * 256, so);
    const u32 sx = WS_X1F + T32p * 28 * 1024;
#pragma unroll
    for (int s = 0; s < 28; ++s) B[s] = ld128c(rWS, lane * 16 + (s & 3) * 1024, sx + (s >> 2) * 4096);
    if (tid < 112) L.w128(L_TCS + (j >> 2) * 256 + kind * 64 + (j & 3) * 16, tcs);
  }
  float rstd2, mr2;
  {
    float mean = 0.f, m2 = 0.f;
#pragma unroll
    for (int i = 0; i < 14; ++i) mean += __uint_as_float(st[i].x);
    mean *= (1.f / 14.f);
#pragma unroll
    for (int i = 0; i < 14; ++i) { const float d = __uint_as_float(st[i].x) - mean; m2 += fmaf(32.f * d, d, __uint_as_float(st[i].y)); }
    rstd2 = __builtin_amdgcn_rsqf(m2 * (1.f / 448.f) + 1e-5f);
    mr2 = -mean * rstd2;
  }
  const u32 so_act = WS_ACT + (T32p * 112 + fs * 7) * 1024;
  auto act_store = [&](const f32x16& a, int tile) {
    u32 o[4];
    sfor<0, 2>([&](auto qq) {
      constexpr int Q = qq.value;
      const u32 tb = L_TCS + tile * 256 + (8 * Q + 4 * h) * 4;
      const f32x4 cg = L.rf4(tb), cu = L.rf4(tb + 64), bg = L.rf4(tb + 128), bu = L.rf4(tb + 192);
      const f32x2 g0 = fma2(pr<4 * Q>(a), bc(rstd2), fma2(lo2(cg), bc(mr2), lo2(bg)));
      const f32x2 g1 = fma2(pr<4 * Q + 2>(a), bc(rstd2), fma2(hi2(cg), bc(mr2), hi2(bg)));
      const f32x2 u0 = fma2(pr<8 + 4 * Q>(a), bc(rstd2), fma2(lo2(cu), bc(mr2), lo2(bu)));
      const f32x2 u1 = fma2(pr<8 + 4 * Q + 2>(a), bc(rstd2), fma2(hi2(cu), bc(mr2), hi2(bu)));
      o[2 * Q] = pkv(g0 * u0); o[2 * Q + 1] = pkv(g1 * u1);
    });
    v4u ov = {o[0], o[1], o[2], o[3]};
    st128c(rWS, lane * 16 + (tile & 3) * 1024, so_act + (tile >> 2) * 4096, ov);
  };
  {
    f32x16 acc[4] = {};
    u32 bA[4];
#pragma unroll
    for (int i = 0; i < 4; ++i) bA[i] = (g16 ? L_AU : L_AG) + (4 * h + q4) * 128 + ((i ^ ((q4 >> 1) & 1) ^ (g16 * 2)) * 32) + p4 * 8;
    v4u f[2][4];
#pragma unroll
    for (int i = 0; i < 4; ++i) f[0][i] = L.tr2(bA[i], bA[i] + 1024);
#pragma unroll
    for (int s = 0; s < 28; ++s) {
#pragma unroll
      for (int i = 0; i < 4; ++i) {
        acc[i] = mfma(f[s & 1][i], B[s], acc[i]);
        if (s + 1 < 28) f[(s + 1) & 1][i] = L.tr2(bA[i] + (s + 1) * 2048, bA[i] + (s + 1) * 2048 + 1024);
        SCHED();
      }
    }
    STAMP(18);
    BAR();
    dma_B(224 * 3584, lds0 + (w < 2 ? L_B2G : L_B2U) + (w & 1) * 1024);
#pragma unroll
    for (int i = 0; i < 4; ++i) act_store(acc[i], i);
  }
  STAMP(19);
  {
    f32x16 acc[3] = {};
    const u32 b1 = (g16 ? L_B1U : L_B1G) + (4 * h + q4) * 96 + p4 * 8;
    const u32 b2 = (g16 ? L_B2U : L_B2G) + (4 * h + q4) * 96 + p4 * 8;
    auto rd = [&](int s, int i) -> v4u {
      const u32 b = s < 14 ? b1 : b2;
      const u32 o = ((s < 14 ? s : s - 14) * 16) * 96 + i * 32;
      return L.tr2(b + o, b + o + 8 * 96);
    };
    v4u f[2][3];
#pragma unroll
    for (int i = 0; i < 3; ++i) f[0][i] = rd(0, i);
#pragma unroll
    for (int s = 0; s < 28; ++s) {
      if (s == 13) { VMW(4); BAR(); }
#pragma unroll
      for (int i = 0; i < 3; ++i) {
        acc[i] = mfma(f[s & 1][i], B[s], acc[i]);
        if (s + 1 < 28) f[(s + 1) & 1][i] = rd(s + 1, i);
        SCHED();
      }
    }
    STAMP(20);
#pragma unroll
    for (int i = 0; i < 3; ++i) act_store(acc[i], 4 + i);
  }
  BAR();
  RELANE();
  const u32 rrD = lane >> 3;
  const u32 vD = rrD * 896 + cs * 128 + (((lane & 7) ^ (((rrD >> 1) & 1) << 2)) * 16);
  const u32 sgD = w * 7168;
  if (p24) {
    dma<18, L_WD0, 4096, 0, 28672>(vD, rWD, sgD, sl_w);
    dma<18, L_WD1, 4096, 896 * 896, 28672>(vD, rWD, sgD, sl_w);
    VMW(36);
  } else {
    VMW(0);
  }
  BAR();
  if (tid == 0) set_flag(rWS, FL_A + (tt * 16 + fs) * 2, tlo, thi);
  STAMP(21);
  if (p24) {
    if (w == 0) wait_flags(rWS, WS_FLAG + (FL_A + ((t64 >> 1) * 16 + (lane & 15)) * 2) * 256, lane < 16, tlo, thi, 6);
    VMW(0);
    BAR();
    STAMP(22);
    const u32 T32 = t64 * 2 + tt2;
    const u32 sa0 = WS_ACT + (T32 * 112 + kh * 56) * 1024;
    const u32 va = lane * 16;
    constexpr int WIN = 24, RF = 20, NRF = 20, RW = 35;
    v4u A_[56];
    auto a_load = [&](auto o) { ald128c<(o.value & 3) * 1024>(A_[o.value], va, rWS, sa0 + (o.value >> 2) * 4096); };
    sfor<0, WIN>(a_load);
    f32x16 acc[2] = {};
    const u32 sw = ((q4 >> 1) & 1) << 2;
    const u32 rb = (kh ? L_WD1 : L_WD0) + (4 * h + q4) * 128 + (p4 & 1) * 8;
    const u32 c0 = rb + (((2 * g16 + (p4 >> 1)) ^ sw) * 16), c1 = rb + (((4 + 2 * g16 + (p4 >> 1)) ^ sw) * 16);
    auto rd = [&](int o, int ct) -> v4u { const u32 b = (ct ? c1 : c0) + (o < 36 ? o : o - 36) * 2048; return L.tr2(b, b + 1024); };
    v4u f[2][2];
    f[0][0] = rd(0, 0); f[0][1] = rd(0, 1);
    sfor<0, 56>([&](auto oc) {
      constexpr int o = oc.value;
      if constexpr (o == RF) {
        BAR();
        dma<10, L_WD0, 4096, 72 * 7168, 28672>(vD, rWD, sgD, sl_w);
        dma<10, L_WD1, 4096, 896 * 896 + 72 * 7168, 28672>(vD, rWD, sgD, sl_w);
      }
      if constexpr (o == RW) { VMW(RW - RF); BAR(); }
      {
        constexpr int last = (o + WIN - 1 < 55 ? o + WIN - 1 : 55);
        constexpr int n = (last - o) + ((o >= RF && o <= RF - 1 + WIN) ? NRF : 0);
        asm volatile("s_waitcnt vmcnt(%1)" : "+v"(A_[o]) : "n"(n) : "memory");
      }
      acc[0] = mfma(f[o & 1][0], A_[o], acc[0]);
      if constexpr (o + 1 < 56) f[(o + 1) & 1][0] = rd(o + 1, 0);
      SCHED();
      acc[1] = mfma(f[o & 1][1], A_[o], acc[1]);
      if constexpr (o + 1 < 56) f[(o + 1) & 1][1] = rd(o + 1, 1);
      if constexpr (o + WIN < 56) a_load(IC<o + WIN>{});
      SCHED();
    });
    STAMP(23);
    BAR();
    {
      const f32x16 snd = kh ? acc[0] : acc[1];
      const u32 xb = w * 4096 + lane * 16;
#pragma unroll
      for (int qq = 0; qq < 4; ++qq) { f32x4 v = {snd[4 * qq], snd[4 * qq + 1], snd[4 * qq + 2], snd[4 * qq + 3]}; L.wf4(xb + qq * 1024, v); }
    }
    BAR();
    {
      const f32x16 mine = kh ? acc[1] : acc[0];
      const u32 xb = (w ^ 2) * 4096 + lane * 16;
      u32 op[8];
      sfor<0, 4>([&](auto qq) {
        constexpr int Q = qq.value;
        const f32x4 o = L.rf4(xb + Q * 1024);
        const f32x4 gb4 = L.rf4(L_TGB + (kh * 32 + 8 * Q + 4 * h) * 4);
        op[2 * Q] = pkv(fma2(lo2(gb4), pr<4 * Q>(mine) + lo2(o), unp(x1p[2 * Q])));
        op[2 * Q + 1] = pkv(fma2(hi2(gb4), pr<4 * Q + 2>(mine) + hi2(o), unp(x1p[2 * Q + 1])));
      });
      const u32 vo = t32 * 896 + 16 * h, so = T32 * 32 * 896 + (cs * 64 + kh * 32) * 2;
#pragma unroll
      for (int pr_ = 0; pr_ < 2; ++pr_) {
        auto s0 = __builtin_amdgcn_permlane32_swap(op[4 * pr_], op[4 * pr_ + 2], false, false);
        auto s1 = __builtin_amdgcn_permlane32_swap(op[4 * pr_ + 1], op[4 * pr_ + 3], false, false);
        v4u o = {s0[0], s1[0], s0[1], s1[1]};
        __builtin_amdgcn_raw_buffer_store_b128(o, rOUT, vo + 32 * pr_, so, 0);
      }
    }
    STAMP(24);
  }
}

extern "C" void kernel_launch(void* const* d_in, const int* in_sizes, int n_in, void* d_out, int out_size, void* d_ws, size_t ws_size,
                              hipStream_t stream) {
  (void)in_sizes; (void)n_in; (void)out_size; (void)ws_size;
  (void)hipFuncSetAttribute((const void*)fused_block, hipFuncAttributeMaxDynamicSharedMemorySize, LDS_BYTES);
  fused_block<<<256, 256, LDS_BYTES, stream>>>((const u16*)d_in[0], (const u16*)d_in[1], (const u16*)d_in[2], (const u16*)d_in[3],
                                               (const u16*)d_in[5], (const u16*)d_in[6], (const u16*)d_in[7], (const u16*)d_in[8],
                                               (const u16*)d_in[9], (u16*)d_out, (unsigned char*)d_ws);
}
```

```cpp
#include <hip/hip_runtime.h>
#include <stdint.h>

typedef unsigned u32;
typedef unsigned long long u64;
typedef unsigned short u16;
typedef u32 v4u __attribute__((ext_vector_type(4)));
typedef u32 v2u __attribute__((ext_vector_type(2)));
typedef float f32x2 __attribute__((ext_vector_type(2)));
typedef float f32x4 __attribute__((ext_vector_type(4)));
typedef float f32x16 __attribute__((ext_vector_type(16)));
typedef __bf16 bf16x2 __attribute__((ext_vector_type(2)));
typedef __bf16 bf16x8 __attribute__((ext_vector_type(8)));
typedef short s16x4 __attribute__((ext_vector_type(4)));
typedef __amdgpu_buffer_rsrc_t rsrc_t;
#define LAS __attribute__((address_space(3)))
#define DI __device__ __forceinline__

constexpr u32 WS_EPOCH = 0;
constexpr u32 WS_TMO = 256;
constexpr u32 WS_FLAG = 4096;
constexpr u32 FL_KV = 0, FL_O = 256, FL_X = 512, FL_A = 1024;
constexpr u32 WS_MODG = 0x80000;
constexpr u32 MODG_REP = 2688 * 8;
constexpr u32 WS_CSB = 0xB0000;
constexpr u32 WS_STAT = 0xC0000;
constexpr u32 WS_KVP = 0x100000;
constexpr u32 WS_OFR = 0x200000;
constexpr u32 WS_X1F = 0x400000;
constexpr u32 WS_ACT = 0x600000;
constexpr u32 WS_DBG = 0xE00000;
constexpr u32 WS_USED = 0x1000000;

constexpr u32 L_XS = 0;
constexpr u32 L_WKV = 0, L_WQ = 57344;
constexpr u32 L_PHI = 86016, L_VFR = 102400;
constexpr u32 L_T1 = 114688;
constexpr u32 L_ST1 = 118272;
constexpr u32 L_WPH = 119296;
constexpr u32 L_P0S = 121344;
constexpr u32 L_CSP = 123392;
constexpr u32 L_CSQ = 131584;
constexpr u32 L_CSF = 135680;
constexpr u32 L_WO = 0, L_XCH = 57344, L_P0C = 57344;
constexpr u32 L_AG = 0, L_AU = 57344;
constexpr u32 L_B1G = 114688, L_B1U = 136320;
constexpr u32 L_B2G = 0, L_B2U = 57472;
constexpr u32 L_KVR = 157824, L_TCS = 157824;
constexpr u32 L_TGA = 161920, L_TG2 = 162176, L_TGB = 162432;
constexpr u32 L_WD0 = 0, L_WD1 = 73728;
constexpr u32 LDS_BYTES = 163840;

constexpr int SPIN_CAP = 40000;

template <int V> struct IC { static constexpr int value = V; };
template <int I, int N, class F> DI void sfor(F&& f) { if constexpr (I < N) { f(IC<I>{}); sfor<I + 1, N>(f); } }

DI rsrc_t mk(const void* p, u32 bytes) { return __builtin_amdgcn_make_buffer_rsrc((void*)p, 0, (int)bytes, 0x00020000); }
DI float bflo(u32 v) { return __uint_as_float(v << 16); }
DI float bfhi(u32 v) { return __uint_as_float(v & 0xffff0000u); }
DI f32x2 unp(u32 v) { f32x2 r = {bflo(v), bfhi(v)}; return r; }
DI u32 pkv(f32x2 v) { return __builtin_bit_cast(u32, __builtin_convertvector(v, bf16x2)); }
DI u32 pk2(float a, float b) { f32x2 v = {a, b}; return pkv(v); }
DI float rbf(float a) { return bflo(pk2(a, a)); }
DI f32x2 bc(float a) { f32x2 r = {a, a}; return r; }
DI f32x2 fma2(f32x2 a, f32x2 b, f32x2 c) { return __builtin_elementwise_fma(a, b, c); }
DI f32x2 lo2(f32x4 v) { f32x2 r = {v[0], v[1]}; return r; }
DI f32x2 hi2(f32x4 v) { f32x2 r = {v[2], v[3]}; return r; }
template <int R> DI f32x2 pr(const f32x16& a) { f32x2 r = {a[R], a[R + 1]}; return r; }
DI float dot2(u32 a, u32 b, float c) { return __builtin_amdgcn_fdot2_f32_bf16(__builtin_bit_cast(bf16x2, a), __builtin_bit_cast(bf16x2, b), c, false); }
DI bf16x8 asb(v4u v) { return __builtin_bit_cast(bf16x8, v); }
DI f32x16 mfma(v4u a, v4u b, f32x16 c) { return __builtin_amdgcn_mfma_f32_32x32x16_bf16(asb(a), asb(b), c, 0, 0, 0); }
DI float half_sum(float v) { auto s = __builtin_amdgcn_permlane32_swap(__float_as_uint(v), __float_as_uint(v), false, false); return __uint_as_float(s[0]) + __uint_as_float(s[1]); }
DI float half_max(float v) { auto s = __builtin_amdgcn_permlane32_swap(__float_as_uint(v), __float_as_uint(v), false, false); return fmaxf(__uint_as_float(s[0]), __uint_as_float(s[1])); }
DI float half_min(float v) { auto s = __builtin_amdgcn_permlane32_swap(__float_as_uint(v), __float_as_uint(v), false, false); return fminf(__uint_as_float(s[0]), __uint_as_float(s[1])); }
DI float half_hi(float v) { auto s = __builtin_amdgcn_permlane32_swap(__float_as_uint(v), __float_as_uint(v), false, false); return __uint_as_float(s[1]); }
template <int CTRL> DI float dpp_add(float v) { return v + __int_as_float(__builtin_amdgcn_update_dpp(0, __float_as_int(v), CTRL, 0xf, 0xf, true)); }
#define ROR2 0x122
#define ROR4 0x124
#define ROR8 0x128
#define BAR() asm volatile("s_waitcnt lgkmcnt(0)\n\ts_barrier" ::: "memory")
#define VMW(n) asm volatile("s_waitcnt vmcnt(%0)" ::"n"(n) : "memory")
#define SCHED() __builtin_amdgcn_sched_barrier(0)

template <int N, int L0, int LS, int G0, int GS> DI void dma(u32 voff, rsrc_t rs, u32 sg, u32 sl) {
  u32 t;
  asm volatile("s_add_u32 m0, %3, %5\n\ts_add_u32 %0, %2, %6\n\tbuffer_load_dwordx4 %1, %4, %0 offen lds\n\t"
               ".rept %7\n\ts_add_u32 m0, m0, %8\n\ts_add_u32 %0, %0, %9\n\tbuffer_load_dwordx4 %1, %4, %0 offen lds\n\t.endr"
               : "=&s"(t) : "v"(voff), "s"(sg), "s"(sl), "s"(rs), "n"(L0), "n"(G0), "n"(N - 1), "n"(LS), "n"(GS) : "memory", "scc");
}
template <int R> DI void dma_x4(u32 voff, rsrc_t rs, u32 sg, u32 sl) {
  u32 t;
  asm volatile("s_add_u32 m0, %3, %5\n\ts_add_u32 %0, %2, %5\n\tbuffer_load_dwordx4 %1, %4, %0 offen lds\n\t"
               "s_add_u32 m0, m0, 896\n\ts_add_u32 %0, %0, 896\n\tbuffer_load_dwordx4 %1, %4, %0 offen lds\n\t"
               "s_add_u32 m0, m0, 13440\n\ts_add_u32 %0, %0, 13440\n\tbuffer_load_dwordx4 %1, %4, %0 offen lds\n\t"
               "s_add_u32 m0, m0, 896\n\ts_add_u32 %0, %0, 896\n\tbuffer_load_dwordx4 %1, %4, %0 offen lds"
               : "=&s"(t) : "v"(voff), "s"(sg), "s"(sl), "s"(rs), "n"(R * 896) : "memory", "scc");
}
template <int L0, int G0> DI void dma3(u32 v0, u32 v1, u32 v2, rsrc_t rs, u32 sg, u32 sl) {
  u32 t;
  asm volatile("s_add_u32 m0, %5, %7\n\ts_add_u32 %0, %4, %8\n\tbuffer_load_dwordx4 %1, %6, %0 offen lds\n\t"
               "s_add_u32 m0, m0, 2048\n\ts_nop 0\n\tbuffer_load_dwordx4 %2, %6, %0 offen lds\n\t"
               "s_add_u32 m0, m0, 2048\n\ts_nop 0\n\tbuffer_load_dwordx4 %3, %6, %0 offen lds"
               : "=&s"(t) : "v"(v0), "v"(v1), "v"(v2), "s"(sg), "s"(sl), "s"(rs), "n"(L0), "n"(G0) : "memory", "scc");
}
template <int IMM> DI void ald128(v4u& d, u32 voff, rsrc_t rs, u32 soff) {
  asm volatile("buffer_load_dwordx4 %0, %1, %2, %3 offen offset:%4" : "=v"(d) : "v"(voff), "s"(rs), "s"(soff), "n"(IMM) : "memory");
}
template <int IMM> DI void ald128c(v4u& d, u32 voff, rsrc_t rs, u32 soff) {
  asm volatile("buffer_load_dwordx4 %0, %1, %2, %3 offen offset:%4 sc1" : "=v"(d) : "v"(voff), "s"(rs), "s"(soff), "n"(IMM) : "memory");
}
DI void ald64(v2u& d, u32 voff, rsrc_t rs, u32 soff) { asm volatile("buffer_load_dwordx2 %0, %1, %2, %3 offen" : "=v"(d) : "v"(voff), "s"(rs), "s"(soff) : "memory"); }
DI void ald64c(v2u& d, u32 voff, rsrc_t rs, u32 soff) { asm volatile("buffer_load_dwordx2 %0, %1, %2, %3 offen sc1" : "=v"(d) : "v"(voff), "s"(rs), "s"(soff) : "memory"); }
#define TIE(x) asm volatile("" : "+v"(x))

DI v4u ld128(rsrc_t r, u32 off, u32 so = 0) { return __builtin_amdgcn_raw_buffer_load_b128(r, off, so, 0); }
DI v4u ld128c(rsrc_t r, u32 off, u32 so = 0) { return __builtin_amdgcn_raw_buffer_load_b128(r, off, so, 16); }
DI v2u ld64(rsrc_t r, u32 off, u32 so = 0) { return __builtin_amdgcn_raw_buffer_load_b64(r, off, so, 0); }
DI v2u ld64c(rsrc_t r, u32 off, u32 so = 0) { return __builtin_amdgcn_raw_buffer_load_b64(r, off, so, 16); }
DI void st128c(rsrc_t r, u32 off, u32 so, v4u v) { __builtin_amdgcn_raw_buffer_store_b128(v, r, off, so, 16); }
DI void st64c(rsrc_t r, u32 off, u32 so, v2u v) { __builtin_amdgcn_raw_buffer_store_b64(v, r, off, so, 16); }
DI void st32c(rsrc_t r, u32 off, u32 so, u32 v) { __builtin_amdgcn_raw_buffer_store_b32(v, r, off, so, 16); }

struct Lds {
  LAS unsigned char* p;
  DI v4u r128(u32 o) const { return *(LAS v4u*)(p + o); }
  DI v2u r64(u32 o) const { return *(LAS v2u*)(p + o); }
  DI f32x4 rf4(u32 o) const { return *(LAS f32x4*)(p + o); }
  DI f32x2 rf2(u32 o) const { return *(LAS f32x2*)(p + o); }
  DI float rf(u32 o) const { return *(LAS float*)(p + o); }
  DI void w128(u32 o, v4u v) const { *(LAS v4u*)(p + o) = v; }
  DI void w64(u32 o, v2u v) const { *(LAS v2u*)(p + o) = v; }
  DI void wf4(u32 o, f32x4 v) const { *(LAS f32x4*)(p + o) = v; }
  DI void wf2(u32 o, f32x2 v) const { *(LAS f32x2*)(p + o) = v; }
  DI void wf(u32 o, float v) const { *(LAS float*)(p + o) = v; }
  DI v2u tr(u32 o) const { return __builtin_bit_cast(v2u, __builtin_amdgcn_ds_read_tr16_b64_v4i16((LAS s16x4*)(p + o))); }
  DI v4u tr2(u32 o0, u32 o1) const { v2u a = tr(o0), b = tr(o1); v4u r = {a.x, a.y, b.x, b.y}; return r; }
};

DI void wait_flags(rsrc_t ws, u32 off, bool active, u32 tlo, u32 thi, u32 code) {
  for (int spins = 0;; ++spins) {
    asm volatile("" ::: "memory");
    v2u v = ld64c(ws, off);
    bool ok = !active || (v.x == tlo && v.y == thi);
    if (__all(ok)) break;
    if (spins > SPIN_CAP) { st32c(ws, WS_TMO, 0, code); break; }
    __builtin_amdgcn_s_sleep(1);
  }
  asm volatile("" ::: "memory");
}
DI void set_flag(rsrc_t ws, u32 idx, u32 tlo, u32 thi) { v2u t = {tlo, thi}; st64c(ws, WS_FLAG + idx * 256, 0, t); }
DI bool gran_ok(v4u a, u32 glo, u32 ghi) { return (a.x & 0xffff0000u) == glo && a.y == ghi && (a.z & 0xffff0000u) == glo && a.w == ghi; }

#define STAMP(i)

DI void feature_map(const f32x16& pin, int h, u32 (&P1)[8], u32 (&P2)[8]) {
  const float L2E = 1.4426950408889634f, INF = __builtin_inff();
  float pm[16], pn[16];
#pragma unroll
  for (int r = 0; r < 16; ++r) { pm[r] = pin[r]; pn[r] = pin[r]; }
#pragma unroll
  for (int r = 12; r < 16; ++r) { pm[r] = h ? -INF : pin[r]; pn[r] = h ? INF : pin[r]; }
  float mx = fmaxf(pm[0], pm[1]), mn = fminf(pn[0], pn[1]);
#pragma unroll
  for (int r = 2; r < 16; r += 2) { mx = fmaxf(fmaxf(mx, pm[r]), pm[r + 1]); mn = fminf(fminf(mn, pn[r]), pn[r + 1]); }
  mx = half_max(mx); mn = half_min(mn);
  const f32x2 cmx = bc(-mx * L2E), cmn = bc(mn * L2E), pl = bc(L2E), nl = bc(-L2E);
  f32x2 e1[8], e2[8];
#pragma unroll
  for (int i = 0; i < 8; ++i) {
    const f32x2 a = {pm[2 * i], pm[2 * i + 1]}, b = {pn[2 * i], pn[2 * i + 1]};
    const f32x2 ta = fma2(a, pl, cmx), tb = fma2(b, nl, cmn);
    e1[i] = f32x2{__builtin_amdgcn_exp2f(ta.x), __builtin_amdgcn_exp2f(ta.y)};
    e2[i] = f32x2{__builtin_amdgcn_exp2f(tb.x), __builtin_amdgcn_exp2f(tb.y)};
  }
  f32x2 s1 = ((e1[0] + e1[1]) + (e1[2] + e1[3])) + ((e1[4] + e1[5]) + (e1[6] + e1[7]));
  f32x2 s2 = ((e2[0] + e2[1]) + (e2[2] + e2[3])) + ((e2[4] + e2[5]) + (e2[6] + e2[7]));
  const f32x2 r1 = bc(__builtin_amdgcn_rcpf(half_sum(s1.x + s1.y))), r2 = bc(__builtin_amdgcn_rcpf(half_sum(s2.x + s2.y)));
#pragma unroll
  for (int i = 0; i < 8; ++i) { P1[i] = pkv(e1[i] * r1); P2[i] = pkv(e2[i] * r2); }
}

template <int NV> DI void gemv_reduce_store(const Lds& L, u32 scratch, f32x2 (&acc)[NV][4], int w, int lane) {
  float r[NV][8];
#pragma unroll
  for (int v = 0; v < NV; ++v)
#pragma unroll
    for (int j = 0; j < 8; ++j) { float a = acc[v][j >> 1][j & 1]; a = dpp_add<ROR2>(a); a = dpp_add<ROR4>(a); r[v][j] = dpp_add<ROR8>(a); }
  if ((lane & 14) == 0) {
    const u32 base = scratch + (w * 4 + (lane >> 4)) * (NV * 64) + (lane & 1) * 32;
#pragma unroll
    for (int v = 0; v < NV; ++v) {
      f32x4 a = {r[v][0], r[v][1], r[v][2], r[v][3]}, b = {r[v][4], r[v][5], r[v][6], r[v][7]};
      L.wf4(base + v * 64, a); L.wf4(base + v * 64 + 16, b);
    }
  }
}
DI float silu_bf(float c) { return rbf(c * __builtin_amdgcn_rcpf(1.f + __builtin_amdgcn_exp2f(-1.4426950408889634f * c))); }

DI v4u scale_row(v4u wv, f32x2 gs, f32x2 (&cs)[4], f32x2 (&bs)[4]) {
  v4u o;
  const f32x2 g = bc(gs.x), s = bc(gs.y);
#pragma unroll
  for (int e = 0; e < 4; ++e) {
    const f32x2 a = unp(wv[e]);
    const f32x2 m = a * g;
    o[e] = pkv(m);
    cs[e] += m;
    bs[e] = fma2(a, s, bs[e]);
  }
  return o;
}

__global__ void __launch_bounds__(256, 2)
fused_block(const u16* __restrict__ gx, const u16* __restrict__ gwqkv, const u16* __restrict__ gwphi, const u16* __restrict__ gwo,
            const u16* __restrict__ gwg, const u16* __restrict__ gwu, const u16* __restrict__ gwd, const u16* __restrict__ gc,
            const u16* __restrict__ gwmod, u16* __restrict__ gout, unsigned char* __restrict__ gws) {
  extern __shared__ __attribute__((aligned(16))) unsigned char smem[];
  asm volatile("" ::"s"(gx), "s"(gwqkv), "s"(gwphi), "s"(gwo), "s"(gwg), "s"(gwu), "s"(gwd), "s"(gc), "s"(gwmod), "s"(gout), "s"(gws));
  Lds L; L.p = (LAS unsigned char*)smem;
  const u32 lds0 = (u32)(uintptr_t)L.p;
  int tid = threadIdx.x, lane = tid & 63;
  const int w = __builtin_amdgcn_readfirstlane(tid >> 6);
  const int bid = blockIdx.x, xq = bid & 7, iq = bid >> 3;
  int h, t32, g16, q4, p4;
#define RELANE() do { asm volatile("" : "+v"(tid)); lane = tid & 63; h = lane >> 5; t32 = lane & 31; g16 = (lane >> 4) & 1; q4 = (lane & 15) >> 2; p4 = lane & 3; } while (0)
  RELANE();

  const rsrc_t rWS = mk(gws, WS_USED);
  const rsrc_t rX = mk(gx, 2048 * 896), rWQKV = mk(gwqkv, 448 * 2688), rWPHI = mk(gwphi, 16 * 784 * 2), rWO = mk(gwo, 448 * 896);
  const rsrc_t rWG = mk(gwg, 448 * 3584), rWU = mk(gwu, 448 * 3584), rWD = mk(gwd, 1792 * 896), rC = mk(gc, 896), rWM = mk(gwmod, 448 * 5376);
  const rsrc_t rOUT = mk(gout, 2048 * 896);

  const int hd = 8 * (xq & 1) + (iq & 7), tt = 4 * (xq >> 1) + (iq >> 3);
  const bool p24 = iq < 28;
  const int t64 = 4 * xq + iq / 7, cs = iq % 7;
  const int chunk = (iq >> 2) * 32 + xq * 4 + (iq & 3);
  const bool p0 = chunk < 168 && iq < 24, crit = iq < 8, p0c = iq < 28;
  const u32 zero = 0;

  u32 cpf = 0;
  {
    u64 pc; u32 clen;
    asm volatile("s_getpc_b64 %0\n.Lfb_code0:\n\ts_mov_b32 %1, .Lfb_code1-.Lfb_code0" : "=s"(pc), "=s"(clen));
    if (w == 0) {
      const u32 o = min((u32)(iq * 32 + (lane & 31)) * 64u, clen - 4u);
      asm volatile("global_load_dword %0, %1, %2" : "=v"(cpf) : "v"(o), "s"(pc) : "memory");
    }
  }
  STAMP(0);
  v2u ep;
  ald64c(ep, zero, rWS, zero);
  v2u cw; v4u wm[4];
  const u32 vgemv = (tid >> 1) * 4;
  auto p0_loads = [&]() {
    ald64(cw, vgemv * 2, rC, zero);
    const u32 vo = vgemv * 5376 + chunk * 32 + (tid & 1) * 16;
    sfor<0, 4>([&](auto j) { ald128<0>(wm[j.value], vo, rWM, j.value * 5376); });
  };
  if (crit) p0_loads();
  if (lane < 56) {
    const u32 sg = (tt * 128 + w * 32) * 896, sl = lds0 + L_XS + w * 28672;
    sfor<0, 8>([&](auto k) { dma_x4<2 * k.value>((lane * 16) ^ (k.value * 16), rX, sg, sl); });
  }
  v4u wkv[14];
  {
    const u32 vo = (tid >> 3) * 2688 + (1 + ((tid >> 2) & 1)) * 896 + hd * 56 + (tid & 3) * 16;
    sfor<0, 14>([&](auto i) { ald128<0>(wkv[i.value], vo, rWQKV, i.value * 32 * 2688); });
  }
  v2u wph;
  ald64(wph, ((tid >> 3) < 28 && (tid & 7) < 7) ? (hd * 784 + (tid >> 3) * 28 + (tid & 7) * 4) * 2 : 0x7ffffff0, rWPHI, zero);
  if (p0 && !crit) p0_loads();

  u32 tlo, thi, glo, ghi;
  auto get_tag = [&]() {
    const u64 E = ((u64)__builtin_amdgcn_readfirstlane(ep.y) << 32) | (u32)__builtin_amdgcn_readfirstlane(ep.x);
    const u64 tag = E + 1;
    tlo = (u32)tag; thi = (u32)(tag >> 32);
    glo = tlo << 16; ghi = (u32)(tag >> 16);
  };
  auto p0_math = [&]() {
    f32x2 acc[1][4] = {};
    const float sv[4] = {silu_bf(bflo(cw.x)), silu_bf(bfhi(cw.x)), silu_bf(bflo(cw.y)), silu_bf(bfhi(cw.y))};
#pragma unroll
    for (int j = 0; j < 4; ++j)
#pragma unroll
      for (int e = 0; e < 4; ++e) acc[0][e] = fma2(unp(wm[j][e]), bc(sv[j]), acc[0][e]);
    gemv_reduce_store<1>(L, L_P0S, acc, w, lane);
    BAR();
    if (w == 0) {
      const int rep = lane >> 3, pr_ = lane & 7;
      f32x2 v = {0.f, 0.f};
#pragma unroll
      for (int gI = 0; gI < 16; ++gI) v += L.rf2(L_P0S + gI * 64 + pr_ * 8);
      float v0 = rbf(v.x), v1 = rbf(v.y);
      if ((chunk >= 28 && chunk < 56) || (chunk >= 112 && chunk < 140)) { v0 = 1.f + v0; v1 = 1.f + v1; }
      const u32 pk = pk2(v0, v1);
      v4u gr = {glo | (pk & 0xffffu), ghi, glo | (pk >> 16), ghi};
      st128c(rWS, rep * MODG_REP + 2 * pr_ * 8, WS_MODG + chunk * 128, gr);
    }
  };
  if (crit) {
    asm volatile("s_waitcnt vmcnt(47)" : "+v"(ep), "+v"(cw), "+v"(wm[0]), "+v"(wm[1]), "+v"(wm[2]), "+v"(wm[3])::"memory");
    get_tag();
    p0_math();
  }
  STAMP(1);

  if (crit) { if (w == 0) VMW(16); else VMW(15); } else if (p0) VMW(20); else VMW(15);
  v4u X[28];
  {
    const u32 gsw = (t32 >> 1) & 7;
    u32 xb[4];
#pragma unroll
    for (int j = 0; j < 4; ++j) xb[j] = L_XS + w * 28672 + t32 * 896 + (((2 * j + h) ^ gsw) * 16);
#pragma unroll
    for (int s = 0; s < 28; ++s) X[s] = L.r128(xb[s & 3] + (s >> 2) * 128);
  }
  float rstd1, mr1;
  {
    float s0 = 0.f, s1 = 0.f, q0 = 0.f, q1 = 0.f;
#pragma unroll
    for (int s = 0; s < 28; ++s) {
      s0 = dot2(X[s][0], 0x3f803f80u, s0); s1 = dot2(X[s][1], 0x3f803f80u, s1);
      s0 = dot2(X[s][2], 0x3f803f80u, s0); s1 = dot2(X[s][3], 0x3f803f80u, s1);
      q0 = dot2(X[s][0], X[s][0], q0); q1 = dot2(X[s][1], X[s][1], q1);
      q0 = dot2(X[s][2], X[s][2], q0); q1 = dot2(X[s][3], X[s][3], q1);
    }
    const float m = half_sum(s0 + s1) * (1.f / 448.f);
    const float var = fmaxf(half_sum(q0 + q1) * (1.f / 448.f) - m * m, 0.f);
    rstd1 = __builtin_amdgcn_rsqf(var + 1e-5f);
    mr1 = -m * rstd1;
    if (h == 0) { f32x2 st = {rstd1, mr1}; L.wf2(L_ST1 + (w * 32 + t32) * 8, st); }
  }
  asm volatile("s_waitcnt vmcnt(0)" : "+v"(ep), "+v"(wph), "+v"(wkv[0]), "+v"(wkv[1]), "+v"(wkv[2]), "+v"(wkv[3]), "+v"(wkv[4]), "+v"(wkv[5]), "+v"(wkv[6])::"memory");
  asm volatile("" : "+v"(cpf), "+v"(wkv[7]), "+v"(wkv[8]), "+v"(wkv[9]), "+v"(wkv[10]), "+v"(wkv[11]), "+v"(wkv[12]), "+v"(wkv[13]));
  L.w64(L_WPH + (tid >> 3) * 64 + (tid & 7) * 8, wph);
  if (!crit) {
    get_tag();
    if (p0) { asm volatile("" : "+v"(cw), "+v"(wm[0]), "+v"(wm[1]), "+v"(wm[2]), "+v"(wm[3])); p0_math(); }
  }
  STAMP(2);

  RELANE();
  {
    const u32 so = WS_MODG + xq * MODG_REP;
    const bool act = tid < 224;
    v4u a, b;
    for (int spins = 0;; ++spins) {
      asm volatile("" ::: "memory");
      a = ld128c(rWS, tid * 16, so); b = ld128c(rWS, tid * 16 + 448 * 8, so);
      const bool ok = !act || (gran_ok(a, glo, ghi) && gran_ok(b, glo, ghi));
      if (__all(ok)) break;
      if (spins > SPIN_CAP) { st32c(rWS, WS_TMO, 0, 1); break; }
      __builtin_amdgcn_s_sleep(2);
    }
    if (act) { f32x4 v = {bflo(b.x), bflo(a.x), bflo(b.z), bflo(a.z)}; L.wf4(L_T1 + tid * 16, v); }
  }
  BAR();
  STAMP(3);

  v4u wq[7];
  {
    f32x2 cs8[4] = {}, bs8[4] = {};
    const u32 r0 = tid >> 3;
    const u32 wb = L_WKV + r0 * 128 + (((tid & 7) ^ (((r0 >> 1) & 1) << 2)) * 16);
#pragma unroll
    for (int i = 0; i < 14; ++i) L.w128(wb + i * 4096, scale_row(wkv[i], L.rf2(L_T1 + r0 * 8 + i * 256), cs8, bs8));
    {
      const u32 vo = (tid >> 2) * 2688 + hd * 56 + (tid & 3) * 16;
      sfor<0, 7>([&](auto i) { ald128<0>(wq[i.value], vo, rWQKV, i.value * 64 * 2688); });
    }
    float c8[8], b8[8];
#pragma unroll
    for (int j = 0; j < 8; ++j) { c8[j] = dpp_add<ROR8>(cs8[j >> 1][j & 1]); b8[j] = dpp_add<ROR8>(bs8[j >> 1][j & 1]); }
    if ((lane & 8) == 0) {
      const u32 base = L_CSP + (w * 4 + (lane >> 4)) * 512 + (lane & 7) * 32;
      f32x4 a = {c8[0], c8[1], c8[2], c8[3]}, b = {c8[4], c8[5], c8[6], c8[7]};
      f32x4 c = {b8[0], b8[1], b8[2], b8[3]}, d = {b8[4], b8[5], b8[6], b8[7]};
      L.wf4(base, a); L.wf4(base + 16, b); L.wf4(base + 256, c); L.wf4(base + 272, d);
    }
  }
  BAR();
  STAMP(4);

  f32x16 accK = {}, accV = {};
  {
    float csum = 0.f;
    const u32 cso = L_CSP + (w >> 1) * 256 + ((w & 1) * 32 + t32) * 4 + h * 8 * 512;
    const u32 sw = ((q4 >> 1) & 1) << 2;
    const u32 rowb = L_WKV + (8 * h + q4) * 128 + (p4 & 1) * 8;
    const u32 bk = rowb + (((2 * g16 + (p4 >> 1)) ^ sw) * 16), bv = rowb + (((4 + 2 * g16 + (p4 >> 1)) ^ sw) * 16);
    v4u fk[2], fv[2];
    fk[0] = L.tr2(bk, bk + 512); fv[0] = L.tr2(bv, bv + 512);
#pragma unroll
    for (int s = 0; s < 28; ++s) {
      accK = mfma(fk[s & 1], X[s], accK);
      if (s + 1 < 28) fk[(s + 1) & 1] = L.tr2(bk + (s + 1) * 2048, bk + (s + 1) * 2048 + 512);
      if (s < 8) csum += L.rf(cso + s * 512);
      SCHED();
      accV = mfma(X[s], fv[s & 1], accV);
      if (s + 1 < 28) fv[(s + 1) & 1] = L.tr2(bv + (s + 1) * 2048, bv + (s + 1) * 2048 + 512);
      if (s == 9) { csum = half_sum(csum); if (h == 0) L.wf(L_CSF + (w >> 1) * 384 + ((w & 1) * 32 + t32) * 4, csum); }
      SCHED();
    }
  }
  asm volatile("s_waitcnt vmcnt(0)" : "+v"(wq[0]), "+v"(wq[1]), "+v"(wq[2]), "+v"(wq[3]), "+v"(wq[4]), "+v"(wq[5]), "+v"(wq[6])::"memory");
  BAR();
  STAMP(5);
  v4u PH[2];
  {
    const u32 b = L_WPH + (4 * h + q4) * 64 + g16 * 32 + p4 * 8;
    PH[0] = L.tr2(b, b + 8 * 64); PH[1] = L.tr2(b + 16 * 64, b + 24 * 64);
  }
  auto ln_epi_t = [&](const f32x16& a, u32 col0, v4u& b0, v4u& b1) {
    u32 o[8];
    sfor<0, 4>([&](auto qq) {
      constexpr int Q = qq.value;
      const f32x4 c4 = L.rf4(L_CSF + (col0 + 8 * Q + 4 * h) * 4), b4 = L.rf4(L_CSF + 384 + (col0 + 8 * Q + 4 * h) * 4);
      o[2 * Q] = pkv(fma2(pr<4 * Q>(a), bc(rstd1), fma2(lo2(c4), bc(mr1), lo2(b4))));
      o[2 * Q + 1] = pkv(fma2(pr<4 * Q + 2>(a), bc(rstd1), fma2(hi2(c4), bc(mr1), hi2(b4))));
    });
    b0 = v4u{o[0], o[1], o[2], o[3]}; b1 = v4u{o[4], o[5], o[6], o[7]};
  };
  {
    v4u b0, b1;
    ln_epi_t(accK, 0, b0, b1);
    f32x16 pk = {};
    pk = mfma(PH[0], b0, pk); pk = mfma(PH[1], b1, pk);
    {
      const float cv_ = L.rf(L_CSF + (32 + t32) * 4), bv_ = L.rf(L_CSF + 384 + (32 + t32) * 4);
      const float keep = t32 < 28 ? 1.f : 0.f, one = t32 == 28 ? 1.f : 0.f;
      u32 vb[8];
#pragma unroll
      for (int qq = 0; qq < 4; ++qq) {
        const u32 so = L_ST1 + (w * 32 + 8 * qq + 4 * h) * 8;
        const f32x4 s01 = L.rf4(so), s23 = L.rf4(so + 16);
        float v[4];
        v[0] = fmaf(accV[4 * qq + 0], s01[0], fmaf(s01[1], cv_, bv_));
        v[1] = fmaf(accV[4 * qq + 1], s01[2], fmaf(s01[3], cv_, bv_));
        v[2] = fmaf(accV[4 * qq + 2], s23[0], fmaf(s23[1], cv_, bv_));
        v[3] = fmaf(accV[4 * qq + 3], s23[2], fmaf(s23[3], cv_, bv_));
#pragma unroll
        for (int e = 0; e < 4; ++e) v[e] = t32 < 28 ? v[e] : one;
        vb[2 * qq] = pk2(v[0], v[1]); vb[2 * qq + 1] = pk2(v[2], v[3]);
      }
      (void)keep;
      v4u a = {vb[0], vb[1], vb[2], vb[3]}, b = {vb[4], vb[5], vb[6], vb[7]};
      L.w128(L_VFR + w * 2048 + lane * 16, a); L.w128(L_VFR + w * 2048 + 1024 + lane * 16, b);
    }
    u32 P1[8], P2[8];
    feature_map(pk, h, P1, P2);
    const u32 swt = (t32 & 7) | ((((t32 >> 3) ^ (t32 >> 1)) & 1) << 3);
    const u32 rb = L_PHI + w * 4096 + t32 * 128;
#pragma unroll
    for (int qq = 0; qq < 4; ++qq) {
      v2u a = {P1[2 * qq], P1[2 * qq + 1]}, b = {P2[2 * qq], P2[2 * qq + 1]};
      L.w64(rb + (((2 * qq + h) ^ swt) * 8), a);
      L.w64(rb + (((8 + 2 * qq + h) ^ swt) * 8), b);
    }
  }
  BAR();
  STAMP(6);
  if (w < 2) {
    f32x16 kv = {};
    u32 ad[4];
#pragma unroll
    for (int su = 0; su < 4; ++su) {
      const u32 t = 8 * su + 4 * h + q4;
      const u32 sw_ = (t & 7) | ((((t >> 3) ^ (t >> 1)) & 1) << 3);
      ad[su] = L_PHI + t * 128 + (((8 * w + 4 * g16 + p4) ^ sw_) * 8);
    }
#pragma unroll
    for (int ww = 0; ww < 4; ++ww)
#pragma unroll
      for (int s = 0; s < 2; ++s) {
        const v4u a = L.tr2(ad[2 * s] + ww * 4096, ad[2 * s + 1] + ww * 4096);
        const v4u b = L.r128(L_VFR + ww * 2048 + s * 1024 + lane * 16);
        kv = mfma(a, b, kv);
      }
    v4u o0 = {pkv(pr<0>(kv)), pkv(pr<2>(kv)), pkv(pr<4>(kv)), pkv(pr<6>(kv))};
    v4u o1 = {pkv(pr<8>(kv)), pkv(pr<10>(kv)), pkv(pr<12>(kv)), pkv(pr<14>(kv))};
    const u32 so = WS_KVP + (hd * 16 + tt) * 4096 + w * 2048;
    st128c(rWS, lane * 16, so, o0); st128c(rWS, lane * 16 + 1024, so, o1);
  }
  STAMP(7);
  RELANE();
  const u32 sl_w = lds0 + w * 1024;
  {
    const u32 rr = lane >> 3, c = (lane & 7) ^ (((rr >> 1) & 1) << 2);
    const u32 voff = rr * 896 + cs * 128 + c * 16, sg = w * 7168;
    if (p24) dma<14, L_WO, 4096, 0, 28672>(voff, rWO, sg, sl_w);
  }
  {
    f32x2 cs8[4] = {}, bs8[4] = {};
    const u32 r0 = tid >> 2;
#pragma unroll
    for (int i = 0; i < 7; ++i) L.w128(L_WQ + r0 * 64 + (tid & 3) * 16 + i * 4096, scale_row(wq[i], L.rf2(L_T1 + r0 * 8 + i * 512), cs8, bs8));
    float c8[8], b8[8];
#pragma unroll
    for (int j = 0; j < 8; ++j) { c8[j] = dpp_add<ROR8>(dpp_add<ROR4>(cs8[j >> 1][j & 1])); b8[j] = dpp_add<ROR8>(dpp_add<ROR4>(bs8[j >> 1][j & 1])); }
    if ((lane & 12) == 0) {
      const u32 base = L_CSQ + (w * 4 + (lane >> 4)) * 256 + (lane & 3) * 32;
      f32x4 a = {c8[0], c8[1], c8[2], c8[3]}, b = {c8[4], c8[5], c8[6], c8[7]};
      f32x4 c = {b8[0], b8[1], b8[2], b8[3]}, d = {b8[4], b8[5], b8[6], b8[7]};
      L.wf4(base, a); L.wf4(base + 16, b); L.wf4(base + 128, c); L.wf4(base + 144, d);
    }
  }
  if (p24) VMW(14); else VMW(0);
  BAR();
  if (tid == 0) set_flag(rWS, FL_KV + hd * 16 + tt, tlo, thi);
  STAMP(8);
  f32x16 accQ = {};
  {
    float csum = 0.f;
    const u32 cso = L_CSQ + (w & 1) * 128 + t32 * 4 + h * 8 * 256;
    const u32 bq = L_WQ + (8 * h + q4) * 64 + g16 * 32 + p4 * 8;
    v4u fq[2];
    fq[0] = L.tr2(bq, bq + 256);
#pragma unroll
    for (int s = 0; s < 28; ++s) {
      accQ = mfma(fq[s & 1], X[s], accQ);
      if (s + 1 < 28) fq[(s + 1) & 1] = L.tr2(bq + (s + 1) * 1024, bq + (s + 1) * 1024 + 256);
      if (s < 8) csum += L.rf(cso + s * 256);
      if (s == 9) { csum = half_sum(csum); if (h == 0 && w < 2) L.wf(L_CSF + w * 384 + (64 + t32) * 4, csum); }
      SCHED();
    }
  }
  if (w == 0) wait_flags(rWS, WS_FLAG + (FL_KV + hd * 16 + (lane & 15)) * 256, lane < 16, tlo, thi, 3);
  BAR();
  STAMP(9);
  RELANE();
  v4u pp[16];
  {
    const u32 so = WS_KVP + hd * 16 * 4096 + w * 1024;
#pragma unroll
    for (int i = 0; i < 16; ++i) pp[i] = ld128c(rWS, lane * 16, so + i * 4096);
  }
  u32 Q1[8], Q2[8];
  {
    v4u b0, b1;
    ln_epi_t(accQ, 64, b0, b1);
    f32x16 pq = {};
    pq = mfma(PH[0], b0, pq); pq = mfma(PH[1], b1, pq);
    feature_map(pq, h, Q1, Q2);
  }
  STAMP(10);
  {
    f32x2 s[4] = {};
    float sv[8] = {};
    u32 selLo, selHi;
    asm("s_mov_b32 %0, 0x3f80" : "=s"(selLo));
    asm("s_mov_b32 %0, 0x3f800000" : "=s"(selHi));
#pragma unroll
    for (int i = 0; i < 16; ++i)
#pragma unroll
      for (int e = 0; e < 4; ++e) { sv[2 * e] = dot2(pp[i][e], selLo, sv[2 * e]); sv[2 * e + 1] = dot2(pp[i][e], selHi, sv[2 * e + 1]); }
    (void)s;
    v4u o = {pk2(sv[0], sv[1]), pk2(sv[2], sv[3]), pk2(sv[4], sv[5]), pk2(sv[6], sv[7])};
    L.w128(L_KVR + w * 1024 + lane * 16, o);
  }
  v4u wc[4], mg2[2], msb[2];
  v2u mtg;
  const u32 so_mod = WS_MODG + xq * MODG_REP;
  const u32 vtg = ((tid >> 6) == 0 ? 896 : ((tid >> 6) == 1 ? 1792 : 2240)) * 8 + (cs * 64 + lane) * 8;
  auto mod_rest_loads = [&]() {
    const u32 vr = (vgemv < 448 ? vgemv : 0) * 8;
    mg2[0] = ld128c(rWS, vr + 1792 * 8, so_mod); mg2[1] = ld128c(rWS, vr + 1792 * 8 + 16, so_mod);
    msb[0] = ld128c(rWS, vr + 1344 * 8, so_mod); msb[1] = ld128c(rWS, vr + 1344 * 8 + 16, so_mod);
    mtg = ld64c(rWS, vtg, so_mod);
  };
  if (p0c) {
    const rsrc_t rW = chunk < 112 ? rWG : rWU;
    const int col0 = 16 * (chunk < 112 ? chunk : chunk - 112);
    const u32 vo = vgemv * 3584 + col0 * 2 + (tid & 1) * 16;
#pragma unroll
    for (int j = 0; j < 4; ++j) wc[j] = ld128(rW, vo, j * 3584);
    mod_rest_loads();
  }
  BAR();
  STAMP(11);
  {
    f32x16 o = {};
    v4u b0 = {Q1[0], Q1[1], Q1[2], Q1[3]}, b1 = {Q1[4], Q1[5], Q1[6], Q1[7]}, b2 = {Q2[0], Q2[1], Q2[2], Q2[3]}, b3 = {Q2[4], Q2[5], Q2[6], Q2[7]};
    o = mfma(L.r128(L_KVR + lane * 16), b0, o);
    o = mfma(L.r128(L_KVR + 1024 + lane * 16), b1, o);
    o = mfma(L.r128(L_KVR + 2048 + lane * 16), b2, o);
    o = mfma(L.r128(L_KVR + 3072 + lane * 16), b3, o);
    const float den = half_hi(o[12]) + 1e-6f;
    const float rd = __builtin_amdgcn_rcpf(den), rd2 = h ? 0.f : rd;
    const f32x2 r1 = bc(rd), r2 = bc(rd2);
    v4u o0 = {pkv(pr<0>(o) * r1), pkv(pr<2>(o) * r1), pkv(pr<4>(o) * r1), pkv(pr<6>(o) * r1)};
    v4u o1 = {pkv(pr<8>(o) * r1), pkv(pr<10>(o) * r1), pkv(pr<12>(o) * r2), pkv(pr<14>(o) * r2)};
    const u32 so = WS_OFR + ((tt * 4 + w) * 32 + hd * 2) * 1024;
    st128c(rWS, lane * 16, so, o0); st128c(rWS, lane * 16 + 1024, so, o1);
  }
  STAMP(12);
  RELANE();
  const int fs = hd;
  const u32 rrA = lane >> 3, pA = (lane & 7) >> 1, eA = lane & 1;
  const u32 vAG = rrA * 3584 + fs * 224 + ((pA ^ ((rrA >> 1) & 1)) * 32) + eA * 16;
  const u32 vAU = rrA * 3584 + fs * 224 + ((pA ^ 2 ^ ((rrA >> 1) & 1)) * 32) + eA * 16;
  const u32 sgA = w * 28672;
  u32 vB[3];
#pragma unroll
  for (int j = 0; j < 3; ++j) { const u32 pI = 128 * j + 64 * (w & 1) + lane; vB[j] = (pI / 6) * 3584 + fs * 224 + 128 + (pI % 6) * 16; }
  const rsrc_t rWB = w < 2 ? rWG : rWU;
  auto dma_B = [&](u32 sg, u32 sl) {
    sfor<0, 3>([&](auto a) { dma3<a.value * 6144, a.value * 64 * 3584>(vB[0], vB[1], vB[2], rWB, sg, sl); });
    dma<1, 9 * 2048, 0, 3 * 64 * 3584, 0>(vB[0], rWB, sg, sl);
    if ((w & 1) == 0) dma<1, 10 * 2048, 0, 3 * 64 * 3584, 0>(vB[1], rWB, sg, sl);
  };
  dma<10, L_AU + 4 * 4096, 4096, 4 * 114688, 114688>(vAU, rWU, sgA, sl_w);
  dma_B(0, lds0 + (w < 2 ? L_B1G : L_B1U) + (w & 1) * 1024);
  if (p0c) {
    for (int spins = 0;; ++spins) {
      const bool ok = gran_ok(mg2[0], glo, ghi) && gran_ok(mg2[1], glo, ghi) && gran_ok(msb[0], glo, ghi) && gran_ok(msb[1], glo, ghi) &&
                      (tid >= 192 || ((mtg.x & 0xffff0000u) == glo && mtg.y == ghi));
      if (__all(ok)) break;
      if (spins > SPIN_CAP) { st32c(rWS, WS_TMO, 0, 2); break; }
      asm volatile("" ::: "memory");
      mod_rest_loads();
    }
    f32x2 acc[2][4] = {};
    const float g2v[4] = {bflo(mg2[0].x), bflo(mg2[0].z), bflo(mg2[1].x), bflo(mg2[1].z)};
    const float sbv[4] = {bflo(msb[0].x), bflo(msb[0].z), bflo(msb[1].x), bflo(msb[1].z)};
#pragma unroll
    for (int j = 0; j < 4; ++j)
#pragma unroll
      for (int e = 0; e < 4; ++e) {
        const f32x2 a = unp(wc[j][e]);
        acc[0][e] = fma2(a, bc(g2v[j]), acc[0][e]);
        acc[1][e] = fma2(a, bc(sbv[j]), acc[1][e]);
      }
    gemv_reduce_store<2>(L, L_P0C, acc, w, lane);
    if (tid < 192) L.wf(L_TGA + tid * 4, bflo(mtg.x));
  }
  if ((w & 1) == 0) VMW(21); else VMW(20);
  BAR();
  if (tid == 0) set_flag(rWS, FL_O + tt * 16 + hd, tlo, thi);
  if (p0c && w == 1 && lane < 32) {
    const int mat = chunk >= 112, col0 = 16 * (chunk - 112 * mat);
    float a = 0.f;
#pragma unroll
    for (int gI = 0; gI < 16; ++gI) a += L.rf(L_P0C + gI * 128 + lane * 4);
    st32c(rWS, (((lane >> 4) * 2) * 1792 + (lane & 15)) * 4, WS_CSB + (mat * 1792 + col0) * 4, __float_as_uint(a));
  }
  STAMP(13);

  RELANE();
  u32 x1p[8] = {};
  const int tt2 = w & 1, kh = w >> 1;
  if (p24) {
    v2u xr[4];
    const u32 T32 = t64 * 2 + tt2;
    {
      const u32 vo = t32 * 896 + 8 * h, so = T32 * 32 * 896 + (cs * 64 + kh * 32) * 2;
#pragma unroll
      for (int qq = 0; qq < 4; ++qq) xr[qq] = ld64(rX, vo + 16 * qq, so);
    }
    if (w == 0) wait_flags(rWS, WS_FLAG + (FL_O + (t64 >> 1) * 16 + (lane & 15)) * 256, lane < 16, tlo, thi, 4);
    BAR();
    if (bid == 0 && tid == 0) { v2u t = {tlo, thi}; st64c(rWS, WS_EPOCH, 0, t); }
    STAMP(15);
    v4u O[16];
    {
      const u32 so = WS_OFR + (T32 * 32 + kh * 16) * 1024;
#pragma unroll
      for (int i = 0; i < 16; ++i) O[i] = ld128c(rWS, lane * 16 + (i & 3) * 1024, so + (i >> 2) * 4096);
    }
    f32x16 acc[2] = {};
    {
      const u32 sw = ((q4 >> 1) & 1) << 2;
      const u32 cb0 = (((2 * g16 + (p4 >> 1)) ^ sw) * 16) + (p4 & 1) * 8, cb1 = (((4 + 2 * g16 + (p4 >> 1)) ^ sw) * 16) + (p4 & 1) * 8;
      const u32 rb = L_WO + kh * 8 * 28 * 128 + q4 * 128;
      const u32 bh0 = rb + 4 * h * 128 + cb0, bh1 = rb + 4 * h * 128 + cb1, bn0 = rb + cb0, bn1 = rb + cb1;
      auto rd = [&](int i, int ct) -> v4u {
        const int hh = i >> 1, s = i & 1;
        const u32 o0 = (28 * hh + 16 * s) * 128, o1 = o0 + 8 * 128;
        const u32 b = ct ? bh1 : bh0, bn = ct ? bn1 : bn0;
        return L.tr2(b + o0, (s ? bn : b) + o1);
      };
      v4u f[2][2];
      f[0][0] = rd(0, 0); f[0][1] = rd(0, 1);
#pragma unroll
      for (int i = 0; i < 16; ++i) {
        acc[0] = mfma(f[i & 1][0], O[i], acc[0]);
        if (i + 1 < 16) f[(i + 1) & 1][0] = rd(i + 1, 0);
        SCHED();
        acc[1] = mfma(f[i & 1][1], O[i], acc[1]);
        if (i + 1 < 16) f[(i + 1) & 1][1] = rd(i + 1, 1);
        SCHED();
      }
    }
    {
      const f32x16 snd = kh ? acc[0] : acc[1];
      const u32 xb = L_XCH + w * 4096 + lane * 16;
#pragma unroll
      for (int qq = 0; qq < 4; ++qq) { f32x4 v = {snd[4 * qq], snd[4 * qq + 1], snd[4 * qq + 2], snd[4 * qq + 3]}; L.wf4(xb + qq * 1024, v); }
    }
    BAR();
    {
      const f32x16 mine = kh ? acc[1] : acc[0];
      const u32 xb = L_XCH + (w ^ 2) * 4096 + lane * 16;
      f32x2 x1[8];
      u32 a2[8];
      f32x2 sum = {0.f, 0.f};
      sfor<0, 4>([&](auto qq) {
        constexpr int Q = qq.value;
        const f32x4 o = L.rf4(xb + Q * 1024);
        const f32x4 ga4 = L.rf4(L_TGA + (kh * 32 + 8 * Q + 4 * h) * 4), g24 = L.rf4(L_TG2 + (kh * 32 + 8 * Q + 4 * h) * 4);
        x1p[2 * Q] = pkv(fma2(lo2(ga4), pr<4 * Q>(mine) + lo2(o), unp(xr[Q].x)));
        x1p[2 * Q + 1] = pkv(fma2(hi2(ga4), pr<4 * Q + 2>(mine) + hi2(o), unp(xr[Q].y)));
        x1[2 * Q] = unp(x1p[2 * Q]); x1[2 * Q + 1] = unp(x1p[2 * Q + 1]);
        a2[2 * Q] = pkv(x1[2 * Q] * lo2(g24)); a2[2 * Q + 1] = pkv(x1[2 * Q + 1] * hi2(g24));
        sum += x1[2 * Q] + x1[2 * Q + 1];
      });
      v4u o0 = {a2[0], a2[1], a2[2], a2[3]}, o1 = {a2[4], a2[5], a2[6], a2[7]};
      const u32 so = WS_X1F + (T32 * 28 + cs * 4 + kh * 2) * 1024;
      st128c(rWS, lane * 16, so, o0); st128c(rWS, lane * 16 + 1024, so, o1);
      const float mean = half_sum(sum.x + sum.y) * (1.f / 32.f);
      f32x2 m2v = {0.f, 0.f};
#pragma unroll
      for (int r = 0; r < 8; ++r) { const f32x2 d = x1[r] - bc(mean); m2v = fma2(d, d, m2v); }
      const float m2 = half_sum(m2v.x + m2v.y);
      if (h == 0) { v2u st = {__float_as_uint(mean), __float_as_uint(m2)}; st64c(rWS, t32 * 8, WS_STAT + (T32 * 14 + cs * 2 + kh) * 256, st); }
    }
  } else {
    BAR();
  }
  dma<14, L_AG, 4096, 0, 114688>(vAG, rWG, sgA, sl_w);
  VMW(14);
  BAR();
  if (p24 && tid == 0) set_flag(rWS, FL_X + t64 * 7 + cs, tlo, thi);
  STAMP(16);
  dma<4, L_AU, 4096, 0, 114688>(vAU, rWU, sgA, sl_w);
  if (w == 0) {
    u32 fo;
    if (lane < 14) fo = (tt * 2 + lane / 7) * 7 + lane % 7;
    else {
      const u32 c2 = lane < 21 ? 7 * fs + (lane - 14) : 112 + 7 * fs + (lane - 21);
      const u32 oiq = (c2 >> 5) * 4 + (c2 & 3), oxq = (c2 >> 2) & 7;
      fo = (4 * oxq + oiq / 7) * 7 + oiq % 7;
    }
    wait_flags(rWS, WS_FLAG + (FL_X + fo) * 256, lane < 28, tlo, thi, 5);
  }
  VMW(0);
  BAR();
  STAMP(17);
  RELANE();
  v4u B[28];
  const u32 T32p = tt * 4 + w;
  v2u st[14];
  v4u tcs;
  {
    const int kind = (tid / 28) & 3, j = tid % 28;
    tcs = ld128c(rWS, (kind * 1792 + j * 4) * 4, WS_CSB + fs * 448);
    const u32 so = WS_STAT + T32p * 14 * 256;
#pragma unroll
    for (int i = 0; i < 14; ++i) st[i] = ld64c(rWS, t32 * 8 + i * 256, so);
    const u32 sx = WS_X1F + T32p * 28 * 1024;
#pragma unroll
    for (int s = 0; s < 28; ++s) B[s] = ld128c(rWS, lane * 16 + (s & 3) * 1024, sx + (s >> 2) * 4096);
    if (tid < 112) L.w128(L_TCS + (j >> 2) * 256 + kind * 64 + (j & 3) * 16, tcs);
  }
  float rstd2, mr2;
  {
    float mean = 0.f, m2 = 0.f;
#pragma unroll
    for (int i = 0; i < 14; ++i) mean += __uint_as_float(st[i].x);
    mean *= (1.f / 14.f);
#pragma unroll
    for (int i = 0; i < 14; ++i) { const float d = __uint_as_float(st[i].x) - mean; m2 += fmaf(32.f * d, d, __uint_as_float(st[i].y)); }
    rstd2 = __builtin_amdgcn_rsqf(m2 * (1.f / 448.f) + 1e-5f);
    mr2 = -mean * rstd2;
  }
  const u32 so_act = WS_ACT + (T32p * 112 + fs * 7) * 1024;
  auto act_store = [&](const f32x16& a, int tile) {
    u32 o[4];
    sfor<0, 2>([&](auto qq) {
      constexpr int Q = qq.value;
      const u32 tb = L_TCS + tile * 256 + (8 * Q + 4 * h) * 4;
      const f32x4 cg = L.rf4(tb), cu = L.rf4(tb + 64), bg = L.rf4(tb + 128), bu = L.rf4(tb + 192);
      const f32x2 g0 = fma2(pr<4 * Q>(a), bc(rstd2), fma2(lo2(cg), bc(mr2), lo2(bg)));
      const f32x2 g1 = fma2(pr<4 * Q + 2>(a), bc(rstd2), fma2(hi2(cg), bc(mr2), hi2(bg)));
      const f32x2 u0 = fma2(pr<8 + 4 * Q>(a), bc(rstd2), fma2(lo2(cu), bc(mr2), lo2(bu)));
      const f32x2 u1 = fma2(pr<8 + 4 * Q + 2>(a), bc(rstd2), fma2(hi2(cu), bc(mr2), hi2(bu)));
      o[2 * Q] = pkv(g0 * u0); o[2 * Q + 1] = pkv(g1 * u1);
    });
    v4u ov = {o[0], o[1], o[2], o[3]};
    st128c(rWS, lane * 16 + (tile & 3) * 1024, so_act + (tile >> 2) * 4096, ov);
  };
  {
    f32x16 acc[4] = {};
    u32 bA[4];
#pragma unroll
    for (int i = 0; i < 4; ++i) bA[i] = (g16 ? L_AU : L_AG) + (4 * h + q4) * 128 + ((i ^ ((q4 >> 1) & 1) ^ (g16 * 2)) * 32) + p4 * 8;
    v4u f[2][4];
#pragma unroll
    for (int i = 0; i < 4; ++i) f[0][i] = L.tr2(bA[i], bA[i] + 1024);
#pragma unroll
    for (int s = 0; s < 28; ++s) {
#pragma unroll
      for (int i = 0; i < 4; ++i) {
        acc[i] = mfma(f[s & 1][i], B[s], acc[i]);
        if (s + 1 < 28) f[(s + 1) & 1][i] = L.tr2(bA[i] + (s + 1) * 2048, bA[i] + (s + 1) * 2048 + 1024);
        SCHED();
      }
    }
    STAMP(18);
    BAR();
    dma_B(224 * 3584, lds0 + (w < 2 ? L_B2G : L_B2U) + (w & 1) * 1024);
#pragma unroll
    for (int i = 0; i < 4; ++i) act_store(acc[i], i);
  }
  STAMP(19);
  {
    f32x16 acc[3] = {};
    const u32 b1 = (g16 ? L_B1U : L_B1G) + (4 * h + q4) * 96 + p4 * 8;
    const u32 b2 = (g16 ? L_B2U : L_B2G) + (4 * h + q4) * 96 + p4 * 8;
    auto rd = [&](int s, int i) -> v4u {
      const u32 b = s < 14 ? b1 : b2;
      const u32 o = ((s < 14 ? s : s - 14) * 16) * 96 + i * 32;
      return L.tr2(b + o, b + o + 8 * 96);
    };
    v4u f[2][3];
#pragma unroll
    for (int i = 0; i < 3; ++i) f[0][i] = rd(0, i);
#pragma unroll
    for (int s = 0; s < 28; ++s) {
      if (s == 13) { VMW(4); BAR(); }
#pragma unroll
      for (int i = 0; i < 3; ++i) {
        acc[i] = mfma(f[s & 1][i], B[s], acc[i]);
        if (s + 1 < 28) f[(s + 1) & 1][i] = rd(s + 1, i);
        SCHED();
      }
    }
    STAMP(20);
#pragma unroll
    for (int i = 0; i < 3; ++i) act_store(acc[i], 4 + i);
  }
  BAR();
  RELANE();
  const u32 rrD = lane >> 3;
  const u32 vD = rrD * 896 + cs * 128 + (((lane & 7) ^ (((rrD >> 1) & 1) << 2)) * 16);
  const u32 sgD = w * 7168;
  if (p24) {
    dma<18, L_WD0, 4096, 0, 28672>(vD, rWD, sgD, sl_w);
    dma<18, L_WD1, 4096, 896 * 896, 28672>(vD, rWD, sgD, sl_w);
    VMW(36);
  } else {
    VMW(0);
  }
  BAR();
  if (tid == 0) set_flag(rWS, FL_A + (tt * 16 + fs) * 2, tlo, thi);
  STAMP(21);
  if (p24) {
    if (w == 0) wait_flags(rWS, WS_FLAG + (FL_A + ((t64 >> 1) * 16 + (lane & 15)) * 2) * 256, lane < 16, tlo, thi, 6);
    VMW(0);
    BAR();
    STAMP(22);
    const u32 T32 = t64 * 2 + tt2;
    const u32 sa0 = WS_ACT + (T32 * 112 + kh * 56) * 1024;
    const u32 va = lane * 16;
    constexpr int WIN = 24, RF = 20, NRF = 20, RW = 35;
    v4u A_[56];
    auto a_load = [&](auto o) { ald128c<(o.value & 3) * 1024>(A_[o.value], va, rWS, sa0 + (o.value >> 2) * 4096); };
    sfor<0, WIN>(a_load);
    f32x16 acc[2] = {};
    const u32 sw = ((q4 >> 1) & 1) << 2;
    const u32 rb = (kh ? L_WD1 : L_WD0) + (4 * h + q4) * 128 + (p4 & 1) * 8;
    const u32 c0 = rb + (((2 * g16 + (p4 >> 1)) ^ sw) * 16), c1 = rb + (((4 + 2 * g16 + (p4 >> 1)) ^ sw) * 16);
    auto rd = [&](int o, int ct) -> v4u { const u32 b = (ct ? c1 : c0) + (o < 36 ? o : o - 36) * 2048; return L.tr2(b, b + 1024); };
    v4u f[2][2];
    f[0][0] = rd(0, 0); f[0][1] = rd(0, 1);
    sfor<0, 56>([&](auto oc) {
      constexpr int o = oc.value;
      if constexpr (o == RF) {
        BAR();
        dma<10, L_WD0, 4096, 72 * 7168, 28672>(vD, rWD, sgD, sl_w);
        dma<10, L_WD1, 4096, 896 * 896 + 72 * 7168, 28672>(vD, rWD, sgD, sl_w);
      }
      if constexpr (o == RW) { VMW(RW - RF); BAR(); }
      {
        constexpr int last = (o + WIN - 1 < 55 ? o + WIN - 1 : 55);
        constexpr int n = (last - o) + ((o >= RF && o <= RF - 1 + WIN) ? NRF : 0);
        asm volatile("s_waitcnt vmcnt(%1)" : "+v"(A_[o]) : "n"(n) : "memory");
      }
      acc[0] = mfma(f[o & 1][0], A_[o], acc[0]);
      if constexpr (o + 1 < 56) f[(o + 1) & 1][0] = rd(o + 1, 0);
      SCHED();
      acc[1] = mfma(f[o & 1][1], A_[o], acc[1]);
      if constexpr (o + 1 < 56) f[(o + 1) & 1][1] = rd(o + 1, 1);
      if constexpr (o + WIN < 56) a_load(IC<o + WIN>{});
      SCHED();
    });
    STAMP(23);
    BAR();
    {
      const f32x16 snd = kh ? acc[0] : acc[1];
      const u32 xb = w * 4096 + lane * 16;
#pragma unroll
      for (int qq = 0; qq < 4; ++qq) { f32x4 v = {snd[4 * qq], snd[4 * qq + 1], snd[4 * qq + 2], snd[4 * qq + 3]}; L.wf4(xb + qq * 1024, v); }
    }
    BAR();
    {
      const f32x16 mine = kh ? acc[1] : acc[0];
      const u32 xb = (w ^ 2) * 4096 + lane * 16;
      u32 op[8];
      sfor<0, 4>([&](auto qq) {
        constexpr int Q = qq.value;
        const f32x4 o = L.rf4(xb + Q * 1024);
        const f32x4 gb4 = L.rf4(L_TGB + (kh * 32 + 8 * Q + 4 * h) * 4);
        op[2 * Q] = pkv(fma2(lo2(gb4), pr<4 * Q>(mine) + lo2(o), unp(x1p[2 * Q])));
        op[2 * Q + 1] = pkv(fma2(hi2(gb4), pr<4 * Q + 2>(mine) + hi2(o), unp(x1p[2 * Q + 1])));
      });
      const u32 vo = t32 * 896 + 16 * h, so = T32 * 32 * 896 + (cs * 64 + kh * 32) * 2;
#pragma unroll
      for (int pr_ = 0; pr_ < 2; ++pr_) {
        auto s0 = __builtin_amdgcn_permlane32_swap(op[4 * pr_], op[4 * pr_ + 2], false, false);
        auto s1 = __builtin_amdgcn_permlane32_swap(op[4 * pr_ + 1], op[4 * pr_ + 3], false, false);
        v4u o = {s0[0], s1[0], s0[1], s1[1]};
        __builtin_amdgcn_raw_buffer_store_b128(o, rOUT, vo + 32 * pr_, so, 0);
      }
    }
    STAMP(24);
    asm volatile(".Lfb_code1:");
  }
}

extern "C" void kernel_launch(void* const* d_in, const int* in_sizes, int n_in, void* d_out, int out_size, void* d_ws, size_t ws_size,
                              hipStream_t stream) {
  (void)in_sizes; (void)n_in; (void)out_size; (void)ws_size;
  (void)hipFuncSetAttribute((const void*)fused_block, hipFuncAttributeMaxDynamicSharedMemorySize, LDS_BYTES);
  fused_block<<<256, 256, LDS_BYTES, stream>>>((const u16*)d_in[0], (const u16*)d_in[1], (const u16*)d_in[2], (const u16*)d_in[3],
                                               (const u16*)d_in[5], (const u16*)d_in[6], (const u16*)d_in[7], (const u16*)d_in[8],
                                               (const u16*)d_in[9], (u16*)d_out, (unsigned char*)d_ws);
}
```

```cpp
#include <hip/hip_runtime.h>
#include <stdint.h>

typedef unsigned u32;
typedef unsigned long long u64;
typedef unsigned short u16;
typedef u32 v4u __attribute__((ext_vector_type(4)));
typedef u32 v2u __attribute__((ext_vector_type(2)));
typedef float f32x2 __attribute__((ext_vector_type(2)));
typedef float f32x4 __attribute__((ext_vector_type(4)));
typedef float f32x16 __attribute__((ext_vector_type(16)));
typedef __bf16 bf16x2 __attribute__((ext_vector_type(2)));
typedef __bf16 bf16x8 __attribute__((ext_vector_type(8)));
typedef short s16x4 __attribute__((ext_vector_type(4)));
typedef __amdgpu_buffer_rsrc_t rsrc_t;
#define LAS __attribute__((address_space(3)))
#define DI __device__ __forceinline__

constexpr u32 WS_EPOCH = 0;
constexpr u32 WS_TMO = 256;
constexpr u32 WS_FLAG = 4096;
constexpr u32 FL_KV = 0, FL_O = 256, FL_X = 512, FL_C = 768, FL_A = 1024;
constexpr u32 WS_MODG = 0x80000;
constexpr u32 MODG_REP = 2688 * 8;
constexpr u32 WS_EXT = 0xB0000;
constexpr u32 WS_STAT = 0xC0000;
constexpr u32 WS_KVP = 0x100000;
constexpr u32 WS_OFR = 0x200000;
constexpr u32 WS_X1F = 0x400000;
constexpr u32 WS_ACT = 0x600000;
constexpr u32 WS_DBG = 0xE00000;
constexpr u32 WS_USED = 0x1000000;

constexpr u32 SLOT = 16384;
constexpr u32 L_XS = 0;
constexpr u32 L_WKV1 = 114688;
constexpr u32 L_WKV2 = 65536;
constexpr u32 L_WQ = 32768;
constexpr u32 L_PHI = 0, L_VFR = 16384;
constexpr u32 L_WO = 65536, L_XCH = 122880, L_XR = 139264;
constexpr u32 L_T1 = 147456;
constexpr u32 L_KVR = 147456;
constexpr u32 L_WPH = 154624;
constexpr u32 L_P0S = 156672;
constexpr u32 L_EXT = 154624;
constexpr u32 L_ST1 = 158720;
constexpr u32 L_TGA = 159232, L_TG2 = 159488, L_TGB = 159744;
constexpr u32 L_CI = 0, L_CT = 147456, L_CB = 149504;
constexpr u32 L_XC4 = 0, L_OUT = 16384;
constexpr u32 LDS_BYTES = 163840;

constexpr int SPIN_CAP = 40000;

template <int V> struct IC { static constexpr int value = V; };
template <int I, int N, class F> DI void sfor(F&& f) { if constexpr (I < N) { f(IC<I>{}); sfor<I + 1, N>(f); } }

DI rsrc_t mk(const void* p, u32 bytes) { return __builtin_amdgcn_make_buffer_rsrc((void*)p, 0, (int)bytes, 0x00020000); }
DI float bflo(u32 v) { return __uint_as_float(v << 16); }
DI float bfhi(u32 v) { return __uint_as_float(v & 0xffff0000u); }
DI f32x2 unp(u32 v) { f32x2 r = {bflo(v), bfhi(v)}; return r; }
DI u32 pkv(f32x2 v) { return __builtin_bit_cast(u32, __builtin_convertvector(v, bf16x2)); }
DI u32 pk2(float a, float b) { f32x2 v = {a, b}; return pkv(v); }
DI float rbf(float a) { return bflo(pk2(a, a)); }
DI f32x2 bc(float a) { f32x2 r = {a, a}; return r; }
DI f32x2 fma2(f32x2 a, f32x2 b, f32x2 c) { return __builtin_elementwise_fma(a, b, c); }
DI f32x2 lo2(f32x4 v) { f32x2 r = {v[0], v[1]}; return r; }
DI f32x2 hi2(f32x4 v) { f32x2 r = {v[2], v[3]}; return r; }
template <int R> DI f32x2 pr(const f32x16& a) { f32x2 r = {a[R], a[R + 1]}; return r; }
DI float dot2(u32 a, u32 b, float c) { return __builtin_amdgcn_fdot2_f32_bf16(__builtin_bit_cast(bf16x2, a), __builtin_bit_cast(bf16x2, b), c, false); }
DI bf16x8 asb(v4u v) { return __builtin_bit_cast(bf16x8, v); }
DI f32x16 mfma(v4u a, v4u b, f32x16 c) { return __builtin_amdgcn_mfma_f32_32x32x16_bf16(asb(a), asb(b), c, 0, 0, 0); }
DI float half_sum(float v) { auto s = __builtin_amdgcn_permlane32_swap(__float_as_uint(v), __float_as_uint(v), false, false); return __uint_as_float(s[0]) + __uint_as_float(s[1]); }
DI float half_max(float v) { auto s = __builtin_amdgcn_permlane32_swap(__float_as_uint(v), __float_as_uint(v), false, false); return fmaxf(__uint_as_float(s[0]), __uint_as_float(s[1])); }
DI float half_min(float v) { auto s = __builtin_amdgcn_permlane32_swap(__float_as_uint(v), __float_as_uint(v), false, false); return fminf(__uint_as_float(s[0]), __uint_as_float(s[1])); }
DI float half_hi(float v) { auto s = __builtin_amdgcn_permlane32_swap(__float_as_uint(v), __float_as_uint(v), false, false); return __uint_as_float(s[1]); }
template <int CTRL> DI float dpp_add(float v) { return v + __int_as_float(__builtin_amdgcn_update_dpp(0, __float_as_int(v), CTRL, 0xf, 0xf, true)); }
#define ROR2 0x122
#define ROR4 0x124
#define ROR8 0x128
#define BAR() asm volatile("s_waitcnt lgkmcnt(0)\n\ts_barrier" ::: "memory")
#define VMW(n) asm volatile("s_waitcnt vmcnt(%0)" ::"n"(n) : "memory")
#define SCHED() __builtin_amdgcn_sched_barrier(0)

template <int N, int L0, int LS, int G0, int GS> DI void dma(u32 voff, rsrc_t rs, u32 sg, u32 sl) {
  u32 t;
  asm volatile("s_add_u32 m0, %3, %5\n\ts_add_u32 %0, %2, %6\n\tbuffer_load_dwordx4 %1, %4, %0 offen lds\n\t"
               ".rept %7\n\ts_add_u32 m0, m0, %8\n\ts_add_u32 %0, %0, %9\n\tbuffer_load_dwordx4 %1, %4, %0 offen lds\n\t.endr"
               : "=&s"(t) : "v"(voff), "s"(sg), "s"(sl), "s"(rs), "n"(L0), "n"(G0), "n"(N - 1), "n"(LS), "n"(GS) : "memory", "scc");
}
template <int R> DI void dma_x4(u32 voff, rsrc_t rs, u32 sg, u32 sl) {
  u32 t;
  asm volatile("s_add_u32 m0, %3, %5\n\ts_add_u32 %0, %2, %5\n\tbuffer_load_dwordx4 %1, %4, %0 offen lds\n\t"
               "s_add_u32 m0, m0, 896\n\ts_add_u32 %0, %0, 896\n\tbuffer_load_dwordx4 %1, %4, %0 offen lds\n\t"
               "s_add_u32 m0, m0, 13440\n\ts_add_u32 %0, %0, 13440\n\tbuffer_load_dwordx4 %1, %4, %0 offen lds\n\t"
               "s_add_u32 m0, m0, 896\n\ts_add_u32 %0, %0, 896\n\tbuffer_load_dwordx4 %1, %4, %0 offen lds"
               : "=&s"(t) : "v"(voff), "s"(sg), "s"(sl), "s"(rs), "n"(R * 896) : "memory", "scc");
}
template <int L0, int G0> DI void dma3(u32 v0, u32 v1, u32 v2, rsrc_t rs, u32 sg, u32 sl) {
  u32 t;
  asm volatile("s_add_u32 m0, %5, %7\n\ts_add_u32 %0, %4, %8\n\tbuffer_load_dwordx4 %1, %6, %0 offen lds\n\t"
               "s_add_u32 m0, m0, 2048\n\ts_nop 0\n\tbuffer_load_dwordx4 %2, %6, %0 offen lds\n\t"
               "s_add_u32 m0, m0, 2048\n\ts_nop 0\n\tbuffer_load_dwordx4 %3, %6, %0 offen lds"
               : "=&s"(t) : "v"(v0), "v"(v1), "v"(v2), "s"(sg), "s"(sl), "s"(rs), "n"(L0), "n"(G0) : "memory", "scc");
}
template <int IMM> DI void ald128(v4u& d, u32 voff, rsrc_t rs, u32 soff) {
  asm volatile("buffer_load_dwordx4 %0, %1, %2, %3 offen offset:%4" : "=v"(d) : "v"(voff), "s"(rs), "s"(soff), "n"(IMM) : "memory");
}
template <int IMM> DI void ald128c(v4u& d, u32 voff, rsrc_t rs, u32 soff) {
  asm volatile("buffer_load_dwordx4 %0, %1, %2, %3 offen offset:%4 sc1" : "=v"(d) : "v"(voff), "s"(rs), "s"(soff), "n"(IMM) : "memory");
}
DI void ald64(v2u& d, u32 voff, rsrc_t rs, u32 soff) { asm volatile("buffer_load_dwordx2 %0, %1, %2, %3 offen" : "=v"(d) : "v"(voff), "s"(rs), "s"(soff) : "memory"); }
DI void ald64c(v2u& d, u32 voff, rsrc_t rs, u32 soff) { asm volatile("buffer_load_dwordx2 %0, %1, %2, %3 offen sc1" : "=v"(d) : "v"(voff), "s"(rs), "s"(soff) : "memory"); }
#define TIE(x) asm volatile("" : "+v"(x))

DI v4u ld128(rsrc_t r, u32 off, u32 so = 0) { return __builtin_amdgcn_raw_buffer_load_b128(r, off, so, 0); }
DI v4u ld128c(rsrc_t r, u32 off, u32 so = 0) { return __builtin_amdgcn_raw_buffer_load_b128(r, off, so, 16); }
DI v2u ld64(rsrc_t r, u32 off, u32 so = 0) { return __builtin_amdgcn_raw_buffer_load_b64(r, off, so, 0); }
DI v2u ld64c(rsrc_t r, u32 off, u32 so = 0) { return __builtin_amdgcn_raw_buffer_load_b64(r, off, so, 16); }
DI void st128c(rsrc_t r, u32 off, u32 so, v4u v) { __builtin_amdgcn_raw_buffer_store_b128(v, r, off, so, 16); }
DI void st64c(rsrc_t r, u32 off, u32 so, v2u v) { __builtin_amdgcn_raw_buffer_store_b64(v, r, off, so, 16); }
DI void st32c(rsrc_t r, u32 off, u32 so, u32 v) { __builtin_amdgcn_raw_buffer_store_b32(v, r, off, so, 16); }

struct Lds {
  LAS unsigned char* p;
  DI v4u r128(u32 o) const { return *(LAS v4u*)(p + o); }
  DI v2u r64(u32 o) const { return *(LAS v2u*)(p + o); }
  DI f32x4 rf4(u32 o) const { return *(LAS f32x4*)(p + o); }
  DI f32x2 rf2(u32 o) const { return *(LAS f32x2*)(p + o); }
  DI float rf(u32 o) const { return *(LAS float*)(p + o); }
  DI void w128(u32 o, v4u v) const { *(LAS v4u*)(p + o) = v; }
  DI void w64(u32 o, v2u v) const { *(LAS v2u*)(p + o) = v; }
  DI void wf4(u32 o, f32x4 v) const { *(LAS f32x4*)(p + o) = v; }
  DI void wf2(u32 o, f32x2 v) const { *(LAS f32x2*)(p + o) = v; }
  DI void wf(u32 o, float v) const { *(LAS float*)(p + o) = v; }
  DI v2u tr(u32 o) const { return __builtin_bit_cast(v2u, __builtin_amdgcn_ds_read_tr16_b64_v4i16((LAS s16x4*)(p + o))); }
  DI v4u tr2(u32 o0, u32 o1) const { v2u a = tr(o0), b = tr(o1); v4u r = {a.x, a.y, b.x, b.y}; return r; }
};

DI void wait_flags(rsrc_t ws, u32 off, bool active, u32 tlo, u32 thi, u32 code) {
  for (int spins = 0;; ++spins) {
    asm volatile("" ::: "memory");
    v2u v = ld64c(ws, off);
    bool ok = !active || (v.x == tlo && v.y == thi);
    if (__all(ok)) break;
    if (spins > SPIN_CAP) { st32c(ws, WS_TMO, 0, code); break; }
    __builtin_amdgcn_s_sleep(1);
  }
  asm volatile("" ::: "memory");
}
DI void set_flag(rsrc_t ws, u32 idx, u32 tlo, u32 thi) { v2u t = {tlo, thi}; st64c(ws, WS_FLAG + idx * 256, 0, t); }
DI bool gran_ok(v4u a, u32 glo, u32 ghi) { return (a.x & 0xffff0000u) == glo && a.y == ghi && (a.z & 0xffff0000u) == glo && a.w == ghi; }

#define STAMP(i)

DI void feature_map(const f32x16& pin, int h, u32 (&P1)[8], u32 (&P2)[8]) {
  const float L2E = 1.4426950408889634f, INF = __builtin_inff();
  float pm[16], pn[16];
#pragma unroll
  for (int r = 0; r < 16; ++r) { pm[r] = pin[r]; pn[r] = pin[r]; }
#pragma unroll
  for (int r = 12; r < 16; ++r) { pm[r] = h ? -INF : pin[r]; pn[r] = h ? INF : pin[r]; }
  float mx = fmaxf(pm[0], pm[1]), mn = fminf(pn[0], pn[1]);
#pragma unroll
  for (int r = 2; r < 16; r += 2) { mx = fmaxf(fmaxf(mx, pm[r]), pm[r + 1]); mn = fminf(fminf(mn, pn[r]), pn[r + 1]); }
  mx = half_max(mx); mn = half_min(mn);
  const f32x2 cmx = bc(-mx * L2E), cmn = bc(mn * L2E), pl = bc(L2E), nl = bc(-L2E);
  f32x2 e1[8], e2[8];
#pragma unroll
  for (int i = 0; i < 8; ++i) {
    const f32x2 a = {pm[2 * i], pm[2 * i + 1]}, b = {pn[2 * i], pn[2 * i + 1]};
    const f32x2 ta = fma2(a, pl, cmx), tb = fma2(b, nl, cmn);
    e1[i] = f32x2{__builtin_amdgcn_exp2f(ta.x), __builtin_amdgcn_exp2f(ta.y)};
    e2[i] = f32x2{__builtin_amdgcn_exp2f(tb.x), __builtin_amdgcn_exp2f(tb.y)};
  }
  f32x2 s1 = ((e1[0] + e1[1]) + (e1[2] + e1[3])) + ((e1[4] + e1[5]) + (e1[6] + e1[7]));
  f32x2 s2 = ((e2[0] + e2[1]) + (e2[2] + e2[3])) + ((e2[4] + e2[5]) + (e2[6] + e2[7]));
  const f32x2 r1 = bc(__builtin_amdgcn_rcpf(half_sum(s1.x + s1.y))), r2 = bc(__builtin_amdgcn_rcpf(half_sum(s2.x + s2.y)));
#pragma unroll
  for (int i = 0; i < 8; ++i) { P1[i] = pkv(e1[i] * r1); P2[i] = pkv(e2[i] * r2); }
}

constexpr int WIN = 24, OB = 20, OPB = 12, NB0 = 12, SST = 8;
struct Plan { int wS, wB[28], wBar[28], wPollA, wAct[56], wPollB; };
constexpr int clampv(int v) { return v > 63 ? 63 : v; }
constexpr Plan mk_plan() {
  Plan P{};
  int n = 0, iPiece[30] = {}, iB[28] = {}, iA[56] = {}, iStore = 0, iPoll = 0;
  for (int i = 0; i < 30; ++i) iPiece[i] = -1000;
  n += 1;
  n += 7; const int iS = n - 1;
  for (int s = 0; s < NB0; ++s) iB[s] = n++;
  for (int G = 0; G < 2; ++G) {
    for (int s = 0; s < 28; ++s) {
      const int pos = G * 7 + s / 4;
      if (G == 0) P.wB[s] = clampv(n - 1 - iB[s]);
      if (G == 0 && s == SST) P.wS = clampv(n - 1 - iS);
      if (G == 1 && s == 24) iPoll = n - 1;
      if (pos >= 1) iPiece[pos + 8] = n++;
      if (G == 0 && s + NB0 < 28) iB[s + NB0] = n++;
      if ((s & 3) == 3) {
        int wv = n - 1 - iPiece[pos + 2];
        if (pos == 8 && n - 1 - iStore < wv) wv = n - 1 - iStore;
        P.wBar[pos] = clampv(wv);
      }
    }
    n += 4; iStore = n - 1;
  }
  P.wPollA = clampv(n - 1 - iPoll);
  for (int j = 0; j < WIN; ++j) iA[j] = n++;
  for (int o = 0; o < 56; ++o) {
    const int pos = 14 + o / 4;
    if (o == OPB) iPoll = n - 1;
    if (o == OB) for (int j = 28; j < 44; ++j) iA[j] = n++;
    P.wAct[o] = clampv(n - 1 - iA[o]);
    if (o < 24) iPiece[pos + 8] = n++;
    if (o < 4) iA[24 + o] = n++;
    else if (o >= OB && o < OB + 12) iA[44 + o - OB] = n++;
    if ((o & 3) == 3) P.wBar[pos] = pos + 2 <= 27 ? clampv(n - 1 - iPiece[pos + 2]) : 63;
    if (o == OB - 1) P.wPollB = clampv(n - 1 - iPoll);
  }
  return P;
}
constexpr Plan PL = mk_plan();
static_assert(WIN == 24 && OB % 4 == 0, "plan");

DI float silu_bf(float c) { return rbf(c * __builtin_amdgcn_rcpf(1.f + __builtin_amdgcn_exp2f(-1.4426950408889634f * c))); }
DI u32 cmask(u32 v, u64 m) { u32 r; asm("v_cndmask_b32_e64 %0, 0, %1, %2" : "=v"(r) : "v"(v), "s"(m)); return r; }
DI u32 opq(u32 v) { asm("" : "+v"(v)); return v; }
DI void split2(float v, u32& hi, u32& lo) { hi = pk2(v, v) & 0xffffu; lo = pk2(v - bflo(hi), 0.f) & 0xffffu; }
#define SBAR() asm volatile("s_barrier" ::: "memory")

__global__ void __launch_bounds__(256, 2)
fused_block(const u16* __restrict__ gx, const u16* __restrict__ gwqkv, const u16* __restrict__ gwphi, const u16* __restrict__ gwo,
            const u16* __restrict__ gwg, const u16* __restrict__ gwu, const u16* __restrict__ gwd, const u16* __restrict__ gc,
            const u16* __restrict__ gwmod, u16* __restrict__ gout, unsigned char* __restrict__ gws) {
  extern __shared__ __attribute__((aligned(16))) unsigned char smem[];
  asm volatile("" ::"s"(gx), "s"(gwqkv), "s"(gwphi), "s"(gwo), "s"(gwg), "s"(gwu), "s"(gwd), "s"(gc), "s"(gwmod), "s"(gout), "s"(gws));
  Lds L; L.p = (LAS unsigned char*)smem;
  const u32 lds0 = (u32)(uintptr_t)L.p;
  int tid = threadIdx.x, lane = tid & 63;
  const int w = __builtin_amdgcn_readfirstlane(tid >> 6);
  const int bid = blockIdx.x, xq = bid & 7, iq = bid >> 3;
  int h, t32, g16, q4, p4;
#define RELANE() do { asm volatile("" : "+v"(tid)); lane = tid & 63; h = lane >> 5; t32 = lane & 31; g16 = (lane >> 4) & 1; q4 = (lane & 15) >> 2; p4 = lane & 3; } while (0)
  RELANE();

  const rsrc_t rWS = mk(gws, WS_USED);
  const rsrc_t rX = mk(gx, 2048 * 896), rWQKV = mk(gwqkv, 448 * 2688), rWPHI = mk(gwphi, 16 * 784 * 2), rWO = mk(gwo, 448 * 896);
  const rsrc_t rWG = mk(gwg, 448 * 3584), rWU = mk(gwu, 448 * 3584), rWD = mk(gwd, 1792 * 896), rC = mk(gc, 896), rWM = mk(gwmod, 448 * 5376);
  const rsrc_t rOUT = mk(gout, 2048 * 896);

  const int hd = 8 * (xq & 1) + (iq & 7), tt = 4 * (xq >> 1) + (iq >> 3);
  const bool p24 = iq < 28;
  const int t64 = 4 * xq + iq / 7, cs = iq % 7;
  const int tt3 = 4 * (xq >> 1) + iq / 7, fs3 = 7 * (xq & 1) + cs;
  const u32 gmask = (xq & 1) ? 0xD5D5D5D5u : 0xABABABABu;
  const int chunk = __builtin_popcount(gmask & ((1u << iq) - 1u)) * 8 + xq;
  const bool p0 = ((gmask >> iq) & 1u) && chunk < 84;
  const u32 zero = 0;

  u32 cpf = 0;
  {
    u64 pc; u32 clen;
    asm volatile("s_getpc_b64 %0\n.Lfb_code0:\n\ts_mov_b32 %1, .Lfb_code1-.Lfb_code0" : "=s"(pc), "=s"(clen));
    if (w == 0) {
      const u32 o = min((u32)(iq * 32 + (lane & 31)) * 64u, clen - 4u);
      asm volatile("global_load_dword %0, %1, %2" : "=v"(cpf) : "v"(o), "s"(pc) : "memory");
    }
  }
  STAMP(0);
  v2u ep;
  ald64c(ep, zero, rWS, zero);
  u32 tlo, thi, glo, ghi;
  auto get_tag = [&]() {
    const u64 E = ((u64)__builtin_amdgcn_readfirstlane(ep.y) << 32) | (u32)__builtin_amdgcn_readfirstlane(ep.x);
    const u64 tag = E + 1;
    tlo = (u32)tag; thi = (u32)(tag >> 32);
    glo = tlo << 16; ghi = (u32)(tag >> 16);
  };
  auto x_dma = [&](auto k0, auto k1) {
    if (lane < 56) {
      const u32 sg = (tt * 128 + w * 32) * 896, sl = lds0 + L_XS + w * 28672;
      sfor<k0.value, k1.value>([&](auto k) { dma_x4<2 * k.value>((lane * 16) ^ (k.value * 16), rX, sg, sl); });
    }
  };
  if (p0) {
    v4u cw, wm[8];
    const u32 r8 = (tid >> 2) * 8;
    ald128<0>(cw, r8 * 2, rC, zero);
    const u32 vo = r8 * 5376 + chunk * 64 + (tid & 3) * 16;
    sfor<0, 8>([&](auto j) { ald128<0>(wm[j.value], vo, rWM, j.value * 5376); });
    x_dma(IC<0>{}, IC<8>{});
    asm volatile("s_waitcnt vmcnt(32)" : "+v"(ep), "+v"(cw), "+v"(wm[0]), "+v"(wm[1]), "+v"(wm[2]), "+v"(wm[3]), "+v"(wm[4]), "+v"(wm[5]), "+v"(wm[6]), "+v"(wm[7])::"memory");
    get_tag();
    f32x2 acc[4] = {};
#pragma unroll
    for (int j = 0; j < 8; ++j) {
      const float sv = silu_bf((j & 1) ? bfhi(cw[j >> 1]) : bflo(cw[j >> 1]));
#pragma unroll
      for (int e = 0; e < 4; ++e) acc[e] = fma2(unp(wm[j][e]), bc(sv), acc[e]);
    }
    float r[8];
#pragma unroll
    for (int j = 0; j < 8; ++j) r[j] = dpp_add<ROR8>(dpp_add<ROR4>(acc[j >> 1][j & 1]));
    if ((lane & 12) == 0) {
      const u32 base = L_P0S + (w * 4 + (lane >> 4)) * 128 + (lane & 3) * 32;
      f32x4 a = {r[0], r[1], r[2], r[3]}, b = {r[4], r[5], r[6], r[7]};
      L.wf4(base, a); L.wf4(base + 16, b);
    }
    BAR();
    if (w < 2) {
      const int rep = w * 4 + (lane >> 4), pr_ = lane & 15;
      f32x2 v = {0.f, 0.f};
#pragma unroll
      for (int gI = 0; gI < 16; ++gI) v += L.rf2(L_P0S + gI * 128 + pr_ * 8);
      float v0 = rbf(v.x), v1 = rbf(v.y);
      if ((chunk >= 14 && chunk < 28) || (chunk >= 56 && chunk < 70)) { v0 = 1.f + v0; v1 = 1.f + v1; }
      const u32 pk = pk2(v0, v1);
      v4u gr = {glo | (pk & 0xffffu), ghi, glo | (pk >> 16), ghi};
      st128c(rWS, rep * MODG_REP + 2 * pr_ * 8, WS_MODG + chunk * 256, gr);
    }
  } else {
    x_dma(IC<0>{}, IC<8>{});
  }
  STAMP(1);
  const u32 vW = (lane >> 2) * 2688 + hd * 56 + min((lane & 3) * 16, 40);
  const u32 sgW = w * 43008;
  sfor<0, 4>([&](auto n) { dma<2, L_WKV1 + n.value * 8192, 1024, n.value * 4 * 43008 + 896, 896>(vW, rWQKV, sgW, lds0 + w * 2048); });
  v2u wph;
  ald64(wph, ((tid >> 3) < 28 && (tid & 7) < 7) ? (hd * 784 + (tid >> 3) * 28 + (tid & 7) * 4) * 2 : 0x7ffffff0, rWPHI, zero);
  v4u ga_, gb_;
  const u32 so_mod = WS_MODG + xq * MODG_REP;
  ald128c<0>(ga_, tid * 16, rWS, so_mod);
  ald128c<448 * 8>(gb_, tid * 16, rWS, so_mod);

  if (p0 && w < 2) VMW(12); else VMW(11);
  asm volatile("" : "+v"(ep), "+v"(cpf)::"memory");
  if (!p0) get_tag();
  v4u X[28];
  {
    const u32 gsw = (t32 >> 1) & 7;
    u32 xb[4];
#pragma unroll
    for (int j = 0; j < 4; ++j) xb[j] = L_XS + w * 28672 + t32 * 896 + (((2 * j + h) ^ gsw) * 16);
#pragma unroll
    for (int s = 0; s < 28; ++s) X[s] = L.r128(xb[s & 3] + (s >> 2) * 128);
  }
  BAR();
  STAMP(2);
  float rstd1;
  v4u Bc;
  {
    float s0 = 0.f, s1 = 0.f, q0 = 0.f, q1 = 0.f;
    sfor<0, 28>([&](auto sc) {
      constexpr int s = sc.value;
      if constexpr (s % 4 == 0 && s < 24) {
        constexpr int n = 4 + s / 8, m = (s / 4) & 1;
        dma<1, L_WKV2 + (n - 4) * 8192 + m * 1024, 0, n * 4 * 43008 + 896 + m * 896, 0>(vW, rWQKV, sgW, lds0 + w * 2048);
        SCHED();
      }
      s0 = dot2(X[s][0], 0x3f803f80u, s0); s1 = dot2(X[s][1], 0x3f803f80u, s1);
      s0 = dot2(X[s][2], 0x3f803f80u, s0); s1 = dot2(X[s][3], 0x3f803f80u, s1);
      q0 = dot2(X[s][0], X[s][0], q0); q1 = dot2(X[s][1], X[s][1], q1);
      q0 = dot2(X[s][2], X[s][2], q0); q1 = dot2(X[s][3], X[s][3], q1);
      if constexpr (s % 4 == 3) SCHED();
    });
    const float m = half_sum(s0 + s1) * (1.f / 448.f);
    const float var = fmaxf(half_sum(q0 + q1) * (1.f / 448.f) - m * m, 0.f) + 1e-5f;
    rstd1 = __builtin_amdgcn_rsqf(var);
    u32 mh, ml, sh, sl_;
    split2(-m, mh, ml); split2(var * rstd1, sh, sl_);
    Bc = h ? v4u{0u, 0u, 0u, 0u} : v4u{mh | (mh << 16), ml | (ml << 16), sh | (sl_ << 16), 0u};
    if (h == 0) L.wf(L_ST1 + (w * 32 + t32) * 4, rstd1);
  }
  asm volatile("s_waitcnt vmcnt(6)" : "+v"(wph), "+v"(ga_), "+v"(gb_)::"memory");
  L.w64(L_WPH + (tid >> 3) * 64 + (tid & 7) * 8, wph);
  STAMP(3);
  {
    const bool act = tid < 224;
    for (int spins = 0;; ++spins) {
      const bool ok = !act || (gran_ok(ga_, glo, ghi) && gran_ok(gb_, glo, ghi));
      if (__all(ok)) break;
      if (spins > SPIN_CAP) { st32c(rWS, WS_TMO, 0, 1); break; }
      asm volatile("" ::: "memory");
      ga_ = ld128c(rWS, tid * 16, so_mod); gb_ = ld128c(rWS, tid * 16 + 448 * 8, so_mod);
    }
    if (act) {
      const u32 g0 = gb_.x & 0xffffu, g1 = gb_.z << 16, s0 = (ga_.x & 0xffffu) * 0x10001u, s1 = (ga_.z & 0xffffu) * 0x10001u;
      L.w128(L_T1 + tid * 32, v4u{g0, g0, s0, s0}); L.w128(L_T1 + tid * 32 + 16, v4u{g1, g1, s1, s1});
    }
  }
  BAR();
  STAMP(4);

  RELANE();
  f32x16 accK = {}, accV = {};
  v4u H[28];
  const u32 coff = g16 * 32 + ((p4 ^ (g16 & (p4 >> 1))) * 8);
  {
    u64 m1[4], m2[4];
#pragma unroll
    for (int d = 0; d < 4; ++d) {
      const bool on = ((t32 >> 3) & 1) == h && ((t32 & 7) >> 1) == d;
      m1[d] = __builtin_amdgcn_ballot_w64(on && t32 < 16);
      m2[d] = __builtin_amdgcn_ballot_w64(on && t32 >= 16);
    }
    const u32 tb = opq(L_T1 + t32 * 16);
    const u32 wb1 = opq(L_WKV1 + (4 * h + q4) * 64 + coff), wb2 = opq(L_WKV2 + (4 * h + q4) * 64 + coff);
    auto rdW = [&](int s, int mat) -> v4u { const u32 b = s < 16 ? wb1 + s * 2048 + mat * 1024 : wb2 + (s - 16) * 2048 + mat * 1024; return L.tr2(b, b + 512); };
    auto mkA = [&](const v4u& ac, const u64 (&m)[4]) -> v4u { v4u a = {cmask(ac.x, m[0]), cmask(ac.x, m[1]), cmask(ac.x, m[2]), cmask(ac.x, m[3])}; return a; };
    const f32x16 Z = {};
    f32x16 D[2];
    v4u Ac[2], A1, A2, fk[2], fv[2];
    Ac[0] = L.r128(tb); Ac[1] = L.r128(tb + 512);
    fk[0] = rdW(0, 0); fv[0] = rdW(0, 1); fk[1] = rdW(1, 0); fv[1] = rdW(1, 1);
#pragma unroll
    for (int p = 0; p < 2; ++p) {
      D[p] = mfma(mkA(Ac[p], m1), X[2 * p], Z);
      D[p] = mfma(mkA(Ac[p], m2), X[2 * p + 1], D[p]);
      D[p] = mfma(Ac[p], Bc, D[p]);
    }
    Ac[0] = L.r128(tb + 2 * 512);
    H[0] = v4u{pkv(pr<0>(D[0])), pkv(pr<2>(D[0])), pkv(pr<4>(D[0])), pkv(pr<6>(D[0]))};
    H[1] = v4u{pkv(pr<8>(D[0])), pkv(pr<10>(D[0])), pkv(pr<12>(D[0])), pkv(pr<14>(D[0]))};
    A1 = mkA(Ac[0], m1);
    SCHED();
    sfor<0, 14>([&](auto pc_) {
      constexpr int p = pc_.value;
      constexpr bool nd = p + 2 < 14, nc = p + 1 < 14, n3 = p + 3 < 14;
      if constexpr (p == 0) STAMP(28);
      if constexpr (p == 7) { STAMP(29); VMW(7); SBAR(); STAMP(30); }
      f32x16& Dn = D[p & 1];
      const f32x16& Dc = D[(p + 1) & 1];
      if constexpr (nd) { Dn = mfma(A1, X[2 * p + 4], Z); SCHED(); A2 = mkA(Ac[p & 1], m2); }
      SCHED();
      accK = mfma(fk[0], H[2 * p], accK);
      SCHED();
      if constexpr (nc) { fk[0] = rdW(2 * p + 2, 0); H[2 * p + 2].x = pkv(pr<0>(Dc)); H[2 * p + 2].y = pkv(pr<2>(Dc)); }
      SCHED();
      if constexpr (nd) Dn = mfma(A2, X[2 * p + 5], Dn);
      SCHED();
      if constexpr (n3) Ac[(p + 1) & 1] = L.r128(tb + (p + 3) * 512);
      if constexpr (nc) { H[2 * p + 2].z = pkv(pr<4>(Dc)); H[2 * p + 2].w = pkv(pr<6>(Dc)); H[2 * p + 3].x = pkv(pr<8>(Dc)); }
      SCHED();
      accV = mfma(H[2 * p], fv[0], accV);
      SCHED();
      if constexpr (nc) { fv[0] = rdW(2 * p + 2, 1); H[2 * p + 3].y = pkv(pr<10>(Dc)); H[2 * p + 3].z = pkv(pr<12>(Dc)); }
      SCHED();
      if constexpr (nd) Dn = mfma(Ac[p & 1], Bc, Dn);
      SCHED();
      if constexpr (n3) A1 = mkA(Ac[(p + 1) & 1], m1);
      SCHED();
      accK = mfma(fk[1], H[2 * p + 1], accK);
      SCHED();
      if constexpr (nc) { fk[1] = rdW(2 * p + 3, 0); H[2 * p + 3].w = pkv(pr<14>(Dc)); }
      if constexpr (p < 7) dma<1, L_WQ + p * 4096, 0, p * 4 * 43008, 0>(vW, rWQKV, sgW, lds0 + w * 1024);
      SCHED();
      accV = mfma(H[2 * p + 1], fv[1], accV);
      SCHED();
      if constexpr (nc) fv[1] = rdW(2 * p + 3, 1);
      SCHED();
    });
  }
  STAMP(5);
  v4u PH[2];
  {
    const u32 b = opq(L_WPH + (4 * h + q4) * 64 + g16 * 32 + p4 * 8);
    PH[0] = L.tr2(b, b + 8 * 64); PH[1] = L.tr2(b + 16 * 64, b + 24 * 64);
  }
  auto epi_t = [&](const f32x16& a, v4u& b0, v4u& b1) {
    const f32x2 r = bc(rstd1);
    b0 = v4u{pkv(pr<0>(a) * r), pkv(pr<2>(a) * r), pkv(pr<4>(a) * r), pkv(pr<6>(a) * r)};
    b1 = v4u{pkv(pr<8>(a) * r), pkv(pr<10>(a) * r), pkv(pr<12>(a) * r), pkv(pr<14>(a) * r)};
  };
  {
    v4u b0, b1;
    epi_t(accK, b0, b1);
    f32x16 pk = {};
    pk = mfma(PH[0], b0, pk); pk = mfma(PH[1], b1, pk);
    {
      const float one = t32 == 28 ? 1.f : 0.f;
      const u32 sb_ = opq(L_ST1 + (w * 32 + 4 * h) * 4);
      u32 vb[8];
#pragma unroll
      for (int qq = 0; qq < 4; ++qq) {
        const f32x4 r4 = L.rf4(sb_ + 32 * qq);
        float v[4];
#pragma unroll
        for (int e = 0; e < 4; ++e) { v[e] = accV[4 * qq + e] * r4[e]; v[e] = t32 < 28 ? v[e] : one; }
        vb[2 * qq] = pk2(v[0], v[1]); vb[2 * qq + 1] = pk2(v[2], v[3]);
      }
      v4u a = {vb[0], vb[1], vb[2], vb[3]}, b = {vb[4], vb[5], vb[6], vb[7]};
      L.w128(L_VFR + w * 2048 + lane * 16, a); L.w128(L_VFR + w * 2048 + 1024 + lane * 16, b);
    }
    u32 P1[8], P2[8];
    feature_map(pk, h, P1, P2);
    const u32 swt = (t32 & 7) | ((((t32 >> 3) ^ (t32 >> 1)) & 1) << 3);
    const u32 rb = L_PHI + w * 4096 + t32 * 128;
#pragma unroll
    for (int qq = 0; qq < 4; ++qq) {
      v2u a = {P1[2 * qq], P1[2 * qq + 1]}, b = {P2[2 * qq], P2[2 * qq + 1]};
      L.w64(rb + (((2 * qq + h) ^ swt) * 8), a);
      L.w64(rb + (((8 + 2 * qq + h) ^ swt) * 8), b);
    }
  }
  VMW(0);
  BAR();
  STAMP(6);
  if (w < 2) {
    f32x16 kv = {};
    u32 ad[4];
#pragma unroll
    for (int su = 0; su < 4; ++su) {
      const u32 t = 8 * su + 4 * h + q4;
      const u32 sw_ = (t & 7) | ((((t >> 3) ^ (t >> 1)) & 1) << 3);
      ad[su] = L_PHI + t * 128 + (((8 * w + 4 * g16 + p4) ^ sw_) * 8);
    }
#pragma unroll
    for (int ww = 0; ww < 4; ++ww)
#pragma unroll
      for (int s = 0; s < 2; ++s) {
        const v4u a = L.tr2(ad[2 * s] + ww * 4096, ad[2 * s + 1] + ww * 4096);
        const v4u b = L.r128(L_VFR + ww * 2048 + s * 1024 + lane * 16);
        kv = mfma(a, b, kv);
      }
    v4u o0 = {pkv(pr<0>(kv)), pkv(pr<2>(kv)), pkv(pr<4>(kv)), pkv(pr<6>(kv))};
    v4u o1 = {pkv(pr<8>(kv)), pkv(pr<10>(kv)), pkv(pr<12>(kv)), pkv(pr<14>(kv))};
    const u32 so = WS_KVP + (hd * 16 + tt) * 4096 + w * 2048;
    st128c(rWS, lane * 16, so, o0); st128c(rWS, lane * 16 + 1024, so, o1);
  }
  STAMP(7);
  RELANE();
  const u32 sl_w = lds0 + w * 1024;
  f32x16 accQ = {};
  {
    const u32 rr = lane >> 3;
    const u32 vO = rr * 896 + cs * 128 + (((lane & 7) ^ (((rr >> 1) & 1) << 2)) * 16), sgO = w * 7168;
    const u32 vR = rr * 896 + cs * 128 + (((lane & 7) ^ (rr >> 1)) * 16), sgR = (t64 * 64 + w * 16) * 896;
    const u32 bq = opq(L_WQ + (4 * h + q4) * 64 + g16 * 32 + ((p4 ^ (g16 & (p4 >> 1))) * 8));
    v4u fq[2];
    fq[0] = L.tr2(bq, bq + 512);
    sfor<0, 28>([&](auto sc) {
      constexpr int s = sc.value;
      accQ = mfma(fq[s & 1], H[s], accQ);
      if constexpr (s + 1 < 28) fq[(s + 1) & 1] = L.tr2(bq + (s + 1) * 1024, bq + (s + 1) * 1024 + 512);
      if constexpr (s < 14) dma<1, L_WO + s * 4096, 0, s * 28672, 0>(vO, rWO, sgO, sl_w);
      if constexpr (s == 14) dma<1, L_XR, 0, 0, 0>(vR, rX, sgR, lds0 + w * 2048);
      if constexpr (s == 15) dma<1, L_XR + 1024, 0, 8 * 896, 0>(vR ^ 64, rX, sgR, lds0 + w * 2048);
      SCHED();
    });
  }
  STAMP(23);
  VMW(16);
  BAR();
  if (tid == 0) set_flag(rWS, FL_KV + hd * 16 + tt, tlo, thi);
  STAMP(8);
  RELANE();
  const u32 rrD = lane >> 3;
  const u32 vW3 = rrD * 3584 + fs3 * 256 + ((((lane & 7) >> 1) ^ ((rrD >> 1) & 1) ^ (w & 2)) * 32) + (lane & 1) * 16;
  const u32 vD = rrD * 896 + cs * 128 + (((lane & 7) ^ (((rrD >> 1) & 1) << 2)) * 16);
  const u32 sg3 = (w & 1) * 32 * 3584, sgD = ((w >> 1) * 896 + (w & 1) * 32) * 896, slR = lds0 + w * 4096;
  const rsrc_t rW3 = w < 2 ? rWG : rWU;
  auto ring = [&](auto nc_, auto jc_, auto cc_) {
    constexpr int n = nc_.value, j = jc_.value, c = cc_.value;
    if constexpr (n < 14) dma<c, (n % 9) * SLOT + j * 1024, 1024, (64 * (n % 7) + 8 * j) * 3584 + (n / 7) * 128, 8 * 3584>(vW3, rW3, sg3, slR);
    else dma<c, (n % 9) * SLOT + j * 1024, 1024, (((n - 14) % 7) * 128 + ((n - 14) / 7) * 64 + 8 * j) * 896, 8 * 896>(vD, rWD, sgD, slR);
  };
  u32 Q1[8], Q2[8];
  {
    ring(IC<0>{}, IC<0>{}, IC<4>{});
    v4u b0, b1;
    epi_t(accQ, b0, b1);
    f32x16 pq = {};
    pq = mfma(PH[0], b0, pq); pq = mfma(PH[1], b1, pq);
    SCHED();
    ring(IC<1>{}, IC<0>{}, IC<4>{});
    feature_map(pq, h, Q1, Q2);
  }
  STAMP(9);
  if (w == 0) wait_flags(rWS, WS_FLAG + (FL_KV + hd * 16 + (lane & 15)) * 256, lane < 16, tlo, thi, 3);
  BAR();
  STAMP(10);
  RELANE();
  {
    v4u pp[16];
    const u32 so = WS_KVP + hd * 16 * 4096 + w * 1024;
#pragma unroll
    for (int i = 0; i < 16; ++i) pp[i] = ld128c(rWS, lane * 16, so + i * 4096);
    const u32 vtg = ((w == 0 ? 896 : (w == 1 ? 1792 : 2240)) + cs * 64 + lane) * 8;
    v2u mtg = ld64c(rWS, vtg, so_mod);
    float sv[8] = {};
    u32 selLo, selHi;
    asm("s_mov_b32 %0, 0x3f80" : "=s"(selLo));
    asm("s_mov_b32 %0, 0x3f800000" : "=s"(selHi));
#pragma unroll
    for (int i = 0; i < 16; ++i)
#pragma unroll
      for (int e = 0; e < 4; ++e) { sv[2 * e] = dot2(pp[i][e], selLo, sv[2 * e]); sv[2 * e + 1] = dot2(pp[i][e], selHi, sv[2 * e + 1]); }
    v4u o = {pk2(sv[0], sv[1]), pk2(sv[2], sv[3]), pk2(sv[4], sv[5]), pk2(sv[6], sv[7])};
    L.w128(L_KVR + w * 1024 + lane * 16, o);
    if (w < 3) {
      for (int spins = 0;; ++spins) {
        if (__all((mtg.x & 0xffff0000u) == glo && mtg.y == ghi)) break;
        if (spins > SPIN_CAP) { st32c(rWS, WS_TMO, 0, 2); break; }
        asm volatile("" ::: "memory");
        mtg = ld64c(rWS, vtg, so_mod);
      }
      L.wf(L_TGA + tid * 4, bflo(mtg.x));
    }
  }
  BAR();
  STAMP(11);
  {
    f32x16 o = {};
    const u32 kb = opq(L_KVR + lane * 16);
    v4u b0 = {Q1[0], Q1[1], Q1[2], Q1[3]}, b1 = {Q1[4], Q1[5], Q1[6], Q1[7]}, b2 = {Q2[0], Q2[1], Q2[2], Q2[3]}, b3 = {Q2[4], Q2[5], Q2[6], Q2[7]};
    o = mfma(L.r128(kb), b0, o);
    o = mfma(L.r128(kb + 1024), b1, o);
    o = mfma(L.r128(kb + 2048), b2, o);
    o = mfma(L.r128(kb + 3072), b3, o);
    const float den = half_hi(o[12]) + 1e-6f;
    const float rd = __builtin_amdgcn_rcpf(den), rd2 = h ? 0.f : rd;
    const f32x2 r1 = bc(rd), r2 = bc(rd2);
    v4u o0 = {pkv(pr<0>(o) * r1), pkv(pr<2>(o) * r1), pkv(pr<4>(o) * r1), pkv(pr<6>(o) * r1)};
    v4u o1 = {pkv(pr<8>(o) * r1), pkv(pr<10>(o) * r1), pkv(pr<12>(o) * r2), pkv(pr<14>(o) * r2)};
    const u32 so = WS_OFR + ((tt * 4 + w) * 32 + hd * 2) * 1024;
    st128c(rWS, lane * 16, so, o0); st128c(rWS, lane * 16 + 1024, so, o1);
  }
  ring(IC<2>{}, IC<0>{}, IC<4>{}); ring(IC<3>{}, IC<0>{}, IC<4>{});
  VMW(8);
  BAR();
  if (tid == 0) set_flag(rWS, FL_O + tt * 16 + hd, tlo, thi);
  STAMP(12);
  RELANE();
  if (!p24) {
    const int rank = (iq - 28) * 4 + (xq >> 1);
    if (rank < 14) {
      const int mat = rank / 7, f3 = 7 * (xq & 1) + rank % 7;
      VMW(0);
      BAR();
      {
        const u32 rr = lane >> 3;
        const u32 v = rr * 3584 + f3 * 256 + (((lane & 7) ^ (((rr >> 1) & 1) << 2)) * 16);
        dma<28, L_CI, 2048, 0, 16 * 3584>(v, mat ? rWU : rWG, (w >> 1) * 128 + (w & 1) * 8 * 3584, lds0 + (w >> 1) * 57344 + (w & 1) * 1024);
      }
      {
        const bool act = tid < 224;
        v4u a, b;
        for (int spins = 0;; ++spins) {
          asm volatile("" ::: "memory");
          a = ld128c(rWS, tid * 16 + 1792 * 8, so_mod); b = ld128c(rWS, tid * 16 + 1344 * 8, so_mod);
          const bool ok = !act || (gran_ok(a, glo, ghi) && gran_ok(b, glo, ghi));
          if (__all(ok)) break;
          if (spins > SPIN_CAP) { st32c(rWS, WS_TMO, 0, 7); break; }
        }
        const u32 kk = (2 * tid) & 15;
        const u32 o = L_CT + (tid >> 3) * 64 + ((kk >> 2) & 1) * 16 + ((kk & 3) + 4 * (kk >> 3)) * 2;
        if (act) {
          *(LAS u32*)(L.p + o) = (a.x & 0xffffu) | (a.z << 16);
          *(LAS u32*)(L.p + o + 32) = (b.x & 0xffffu) | (b.z << 16);
        }
      }
      VMW(0);
      BAR();
      f32x16 acc = {};
      const u32 sw = ((q4 >> 1) & 1) << 2;
      const u32 cb = opq(L_CI + (w >> 1) * 57344 + (4 * h + q4) * 128 + (((4 * (w & 1) + 2 * g16 + (p4 >> 1)) ^ sw) * 16) + (p4 & 1) * 8);
      const u32 tb = opq(L_CT + (lane & 1) * 32 + h * 16);
#pragma unroll
      for (int s = 0; s < 28; ++s) acc = mfma(L.tr2(cb + s * 2048, cb + s * 2048 + 1024), L.r128(tb + s * 64), acc);
      if (t32 < 2) {
#pragma unroll
        for (int qq = 0; qq < 4; ++qq) {
          f32x4 v = {acc[4 * qq], acc[4 * qq + 1], acc[4 * qq + 2], acc[4 * qq + 3]};
          L.wf4(L_CB + t32 * 512 + (w * 32 + 8 * qq + 4 * h) * 4, v);
        }
      }
      BAR();
      if (tid < 128) {
        u32 ch, cl, bh, bl;
        split2(L.rf(L_CB + tid * 4), ch, cl); split2(L.rf(L_CB + 512 + tid * 4), bh, bl);
        v4u e = {ch | (cl << 16), ch | (bh << 16), bl | (bh << 16), 0u};
        st128c(rWS, (tid >> 4) * 512 + (tid & 15) * 16, WS_EXT + f3 * 4096 + mat * 256, e);
      }
      VMW(0);
      BAR();
      if (tid == 0) set_flag(rWS, FL_C + mat * 14 + f3, tlo, thi);
    }
    return;
  }

  u32 x1p[8];
  const int tt2 = w & 1, kh = w >> 1;
  const u32 T32 = t64 * 2 + tt2;
  {
    if (w == 0) wait_flags(rWS, WS_FLAG + (FL_O + (t64 >> 1) * 16 + (lane & 15)) * 256, lane < 16, tlo, thi, 4);
    BAR();
    if (bid == 0 && tid == 0) { v2u t = {tlo, thi}; st64c(rWS, WS_EPOCH, 0, t); }
    STAMP(13);
    v4u O[16];
    {
      const u32 so = WS_OFR + (T32 * 32 + kh * 16) * 1024;
#pragma unroll
      for (int i = 0; i < 16; ++i) O[i] = ld128c(rWS, lane * 16 + (i & 3) * 1024, so + (i >> 2) * 4096);
    }
    f32x16 acc[2] = {};
    {
      const u32 sw = ((q4 >> 1) & 1) << 2;
      const u32 cb0 = (((2 * g16 + (p4 >> 1)) ^ sw) * 16) + (p4 & 1) * 8, cb1 = (((4 + 2 * g16 + (p4 >> 1)) ^ sw) * 16) + (p4 & 1) * 8;
      const u32 rb = L_WO + kh * 8 * 28 * 128 + q4 * 128;
      const u32 bh0 = opq(rb + 4 * h * 128 + cb0), bh1 = opq(rb + 4 * h * 128 + cb1), bn0 = opq(rb + cb0), bn1 = opq(rb + cb1);
      auto rd = [&](int i, int ct) -> v4u {
        const int hh = i >> 1, s = i & 1;
        const u32 o0 = (28 * hh + 16 * s) * 128, o1 = o0 + 8 * 128;
        const u32 b = ct ? bh1 : bh0, bn = ct ? bn1 : bn0;
        return L.tr2(b + o0, (s ? bn : b) + o1);
      };
      v4u f[2][2];
      f[0][0] = rd(0, 0); f[0][1] = rd(0, 1);
#pragma unroll
      for (int i = 0; i < 16; ++i) {
        acc[0] = mfma(f[i & 1][0], O[i], acc[0]);
        if (i + 1 < 16) f[(i + 1) & 1][0] = rd(i + 1, 0);
        SCHED();
        acc[1] = mfma(f[i & 1][1], O[i], acc[1]);
        if (i + 1 < 16) f[(i + 1) & 1][1] = rd(i + 1, 1);
        SCHED();
      }
    }
    STAMP(14);
    {
      const f32x16 snd = kh ? acc[0] : acc[1];
      const u32 xb = opq(L_XCH + w * 4096 + lane * 16);
#pragma unroll
      for (int qq = 0; qq < 4; ++qq) { f32x4 v = {snd[4 * qq], snd[4 * qq + 1], snd[4 * qq + 2], snd[4 * qq + 3]}; L.wf4(xb + qq * 1024, v); }
    }
    BAR();
    {
      const f32x16 mine = kh ? acc[1] : acc[0];
      const u32 xb = opq(L_XCH + (w ^ 2) * 4096 + lane * 16);
      const u32 row = tt2 * 32 + t32, rsw = (row >> 1) & 7;
      const u32 xrb = opq(L_XR + row * 128 + 8 * h), tg = opq(L_TGA + (kh * 32 + 4 * h) * 4);
      f32x2 x1[8];
      u32 a2[8];
      f32x2 sum = {0.f, 0.f};
      sfor<0, 4>([&](auto qq) {
        constexpr int Q = qq.value;
        const f32x4 o = L.rf4(xb + Q * 1024);
        const v2u xr = L.r64(xrb + (((kh * 4 + Q) ^ rsw) * 16));
        const f32x4 ga4 = L.rf4(tg + 32 * Q), g24 = L.rf4(tg + 256 + 32 * Q);
        x1p[2 * Q] = pkv(fma2(lo2(ga4), pr<4 * Q>(mine) + lo2(o), unp(xr.x)));
        x1p[2 * Q + 1] = pkv(fma2(hi2(ga4), pr<4 * Q + 2>(mine) + hi2(o), unp(xr.y)));
        x1[2 * Q] = unp(x1p[2 * Q]); x1[2 * Q + 1] = unp(x1p[2 * Q + 1]);
        a2[2 * Q] = pkv(x1[2 * Q] * lo2(g24)); a2[2 * Q + 1] = pkv(x1[2 * Q + 1] * hi2(g24));
        sum += x1[2 * Q] + x1[2 * Q + 1];
      });
      v4u o0 = {a2[0], a2[1], a2[2], a2[3]}, o1 = {a2[4], a2[5], a2[6], a2[7]};
      const u32 so = WS_X1F + (T32 * 28 + cs * 4 + kh * 2) * 1024;
      st128c(rWS, lane * 16, so, o0); st128c(rWS, lane * 16 + 1024, so, o1);
      const float mean = half_sum(sum.x + sum.y) * (1.f / 32.f);
      f32x2 m2v = {0.f, 0.f};
#pragma unroll
      for (int r = 0; r < 8; ++r) { const f32x2 d = x1[r] - bc(mean); m2v = fma2(d, d, m2v); }
      const float m2 = half_sum(m2v.x + m2v.y);
      if (h == 0) { v2u st = {__float_as_uint(mean), __float_as_uint(m2)}; st64c(rWS, t32 * 8, WS_STAT + (T32 * 14 + cs * 2 + kh) * 256, st); }
    }
  }
  ring(IC<4>{}, IC<0>{}, IC<4>{}); ring(IC<5>{}, IC<0>{}, IC<4>{}); ring(IC<6>{}, IC<0>{}, IC<4>{});
  VMW(12);
  BAR();
  if (tid == 0) set_flag(rWS, FL_X + t64 * 7 + cs, tlo, thi);
  STAMP(15);
  ring(IC<7>{}, IC<0>{}, IC<4>{}); ring(IC<8>{}, IC<0>{}, IC<4>{});
  if (w == 0) {
    const u32 fo = lane < 14 ? FL_X + (tt3 * 2 + lane / 7) * 7 + lane % 7 : FL_C + (lane - 14) * 14 + fs3;
    wait_flags(rWS, WS_FLAG + fo * 256, lane < 16, tlo, thi, 5);
  }
  BAR();
  STAMP(16);
  RELANE();
  v4u B[28];
  const u32 T32p = tt3 * 4 + w;
  v2u st[7];
  const u32 sx = WS_X1F + T32p * 28 * 1024, vx = lane * 16;
  dma<1, L_EXT, 0, WS_EXT, 0>(lane * 16, rWS, fs3 * 4096 + w * 1024, lds0 + w * 1024);
  {
    const u32 vs = t32 * 8 + h * 7 * 256, so = WS_STAT + T32p * 14 * 256;
    sfor<0, 7>([&](auto i) { ald64c(st[i.value], vs, rWS, so + i.value * 256); });
    sfor<0, NB0>([&](auto s) { ald128c<(s.value & 3) * 1024>(B[s.value], vx, rWS, sx + (s.value >> 2) * 4096); });
  }
  float rs2;
  v4u Bx;
  auto ln2_stats = [&]() {
    asm volatile("s_waitcnt vmcnt(%7)" : "+v"(st[0]), "+v"(st[1]), "+v"(st[2]), "+v"(st[3]), "+v"(st[4]), "+v"(st[5]), "+v"(st[6]) : "n"(PL.wS) : "memory");
    float mean = 0.f, m2 = 0.f;
#pragma unroll
    for (int i = 0; i < 7; ++i) mean += __uint_as_float(st[i].x);
    mean = half_sum(mean) * (1.f / 14.f);
#pragma unroll
    for (int i = 0; i < 7; ++i) { const float d = __uint_as_float(st[i].x) - mean; m2 += fmaf(32.f * d, d, __uint_as_float(st[i].y)); }
    const float var = half_sum(m2) * (1.f / 448.f) + 1e-5f;
    const float rstd2 = __builtin_amdgcn_rsqf(var);
    rs2 = rstd2 * rstd2;
    u32 mh, ml, sh, sl_;
    split2(-mean, mh, ml); split2(var * rstd2, sh, sl_);
    Bx = h ? v4u{0u, 0u, 0u, 0u} : v4u{mh | (mh << 16), ml | (sh << 16), sh | (sl_ << 16), 0u};
  };
  STAMP(17);
  v2u fa;
  {
    const u32 so_act = WS_ACT + (T32p * 112 + fs3 * 8) * 1024;
    u32 bA[4], bo[4];
#pragma unroll
    for (int i = 0; i < 4; ++i) bo[i] = g16 * 8192 + (4 * h + q4) * 128 + ((i ^ ((q4 >> 1) & 1) ^ (g16 * 2)) * 32) + p4 * 8;
    const u32 eb = opq(L_EXT + t32 * 16);
    int curgrp = -1;
    (void)curgrp;
    v4u f[2][4];
    auto setbase = [&](auto gc_) {
#pragma unroll
      for (int i = 0; i < 4; ++i) bA[i] = opq(bo[i] + gc_.value * 65536);
    };
    auto rdf = [&](auto Gc, auto sc, int i) -> v4u {
      constexpr int G = Gc.value, s = sc.value;
      if constexpr (s == 28) return L.r128(eb + (G * 4 + i) * 512);
      else {
        constexpr int slot = (G * 7 + s / 4) % 9;
        const u32 b = bA[i] + (slot & 3) * SLOT + (s & 3) * 2048;
        return L.tr2(b, b + 1024);
      }
    };
    setbase(IC<0>{});
#pragma unroll
    for (int i = 0; i < 4; ++i) f[0][i] = rdf(IC<0>{}, IC<0>{}, i);
    sfor<0, 2>([&](auto Gc) {
      constexpr int G = Gc.value;
      f32x16 acc[4] = {};
      sfor<0, 29>([&](auto sc) {
        constexpr int s = sc.value, par = (G * 29 + s) & 1;
        constexpr int pos = G * 7 + (s < 28 ? s / 4 : 6);
        constexpr int nG = s + 1 < 29 ? G : G + 1, ns = s + 1 < 29 ? s + 1 : 0;
        constexpr bool nx = nG < 2;
        if constexpr (nx && ns < 28) {
          constexpr int sl_n = (nG * 7 + ns / 4) % 9, sl_p = (ns == 0) ? (G * 7 + 6) % 9 : (nG * 7 + (ns - 1) / 4) % 9;
          if constexpr ((sl_n >> 2) != (sl_p >> 2)) setbase(IC<(sl_n >> 2)>{});
        }
        if constexpr (G == 0 && s < 28) asm volatile("s_waitcnt vmcnt(%1)" : "+v"(B[s]) : "n"(PL.wB[s]) : "memory");
        if constexpr (G == 1 && s == 24) {
          if (w == 0) ald64c(fa, WS_FLAG + (FL_A + ((t64 >> 1) * 14 + (lane < 14 ? lane : 0)) * 2) * 256, rWS, zero);
        }
#pragma unroll
        for (int i = 0; i < 4; ++i) {
          if constexpr (s < 28) acc[i] = mfma(f[par][i], B[s], acc[i]); else acc[i] = mfma(f[par][i], Bx, acc[i]);
          if constexpr (nx) f[par ^ 1][i] = rdf(IC<nG>{}, IC<ns>{}, i);
          if constexpr (s < 28 && pos >= 1) { if (i == 0) ring(IC<pos + 8>{}, IC<(s & 3)>{}, IC<1>{}); }
          if constexpr (G == 0 && s + NB0 < 28) { if (i == 2) ald128c<((s + NB0) & 3) * 1024>(B[s + NB0], vx, rWS, sx + ((s + NB0) >> 2) * 4096); }
          if constexpr (G == 0 && s == SST) { if (i == 3) ln2_stats(); }
          SCHED();
        }
        if constexpr (s < 28 && (s & 3) == 3) {
          if constexpr (PL.wBar[pos] < 63) VMW(PL.wBar[pos]);
          SBAR();
          if constexpr (pos == 8) { if (tid == 0) set_flag(rWS, FL_A + (tt3 * 14 + fs3) * 2, tlo, thi); }
        }
      });
      const f32x2 r2 = bc(rs2);
#pragma unroll
      for (int i = 0; i < 4; ++i) {
        const f32x16& a = acc[i];
        v4u ov = {pkv(pr<0>(a) * pr<8>(a) * r2), pkv(pr<2>(a) * pr<10>(a) * r2), pkv(pr<4>(a) * pr<12>(a) * r2), pkv(pr<6>(a) * pr<14>(a) * r2)};
        st128c(rWS, lane * 16 + i * 1024, so_act + G * 4096, ov);
      }
      STAMP(18 + G);
    });
  }
  RELANE();
  {
    if (w == 0) {
      asm volatile("s_waitcnt vmcnt(%1)" : "+v"(fa) : "n"(PL.wPollA) : "memory");
      if (!__all(fa.x == tlo && fa.y == thi))
        wait_flags(rWS, WS_FLAG + (FL_A + ((t64 >> 1) * 14 + (lane < 14 ? lane : 0)) * 2) * 256, lane < 14, tlo, thi, 6);
    }
    SBAR();
    STAMP(20);
    const u32 sa0 = WS_ACT + (T32 * 112 + kh * 56) * 1024;
    const u32 va = lane * 16;
    v4u A_[56];
    auto a_load = [&](auto jc) {
      constexpr int j = jc.value;
      ald128c<(j & 3) * 1024>(A_[j], va, rWS, sa0 + ((j % 28) >> 2) * 8192 + (j / 28) * 4096);
    };
    sfor<0, WIN>(a_load);
    f32x16 acc[2] = {};
    const u32 sw = ((q4 >> 1) & 1) << 2;
    const u32 rb = kh * 8192 + (4 * h + q4) * 128 + (p4 & 1) * 8;
    const u32 co0 = rb + (((2 * g16 + (p4 >> 1)) ^ sw) * 16), co1 = rb + (((4 + 2 * g16 + (p4 >> 1)) ^ sw) * 16);
    u32 c0, c1;
    auto setb = [&](auto gc_) { c0 = opq(co0 + gc_.value * 65536); c1 = opq(co1 + gc_.value * 65536); };
    auto rd = [&](auto oc, int ct) -> v4u {
      constexpr int o = oc.value, slot = (14 + o / 4) % 9;
      const u32 b = (ct ? c1 : c0) + (slot & 3) * SLOT + (o & 3) * 2048;
      return L.tr2(b, b + 1024);
    };
    setb(IC<((14 % 9) >> 2)>{});
    v4u f[2][2];
    f[0][0] = rd(IC<0>{}, 0); f[0][1] = rd(IC<0>{}, 1);
    v2u fb;
    sfor<0, 56>([&](auto oc) {
      constexpr int o = oc.value, pos = 14 + o / 4;
      if constexpr (o + 1 < 56) {
        constexpr int sl_n = (14 + (o + 1) / 4) % 9, sl_p = pos % 9;
        if constexpr ((sl_n >> 2) != (sl_p >> 2)) setb(IC<(sl_n >> 2)>{});
      }
      if constexpr (o == OPB) {
        if (w == 0) ald64c(fb, WS_FLAG + (FL_A + ((t64 >> 1) * 14 + (lane < 14 ? lane : 0)) * 2 + 1) * 256, rWS, zero);
      }
      if constexpr (o == 4) STAMP(24);
      if constexpr (o == OB) { STAMP(25); sfor<28, 44>(a_load); }
      if constexpr (o == 28) STAMP(26);
      if constexpr (o == 44) STAMP(27);
      asm volatile("s_waitcnt vmcnt(%1)" : "+v"(A_[o]) : "n"(PL.wAct[o]) : "memory");
      acc[0] = mfma(f[o & 1][0], A_[o], acc[0]);
      if constexpr (o + 1 < 56) f[(o + 1) & 1][0] = rd(IC<o + 1>{}, 0);
      if constexpr (o < 24) ring(IC<pos + 8>{}, IC<(o & 3)>{}, IC<1>{});
      SCHED();
      acc[1] = mfma(f[o & 1][1], A_[o], acc[1]);
      if constexpr (o + 1 < 56) f[(o + 1) & 1][1] = rd(IC<o + 1>{}, 1);
      if constexpr (o < 4) a_load(IC<24 + o>{});
      else if constexpr (o >= OB && o < OB + 12) a_load(IC<44 + o - OB>{});
      SCHED();
      if constexpr ((o & 3) == 3 && pos <= 25) {
        if constexpr (o == OB - 1) {
          if (w == 0) {
            asm volatile("s_waitcnt vmcnt(%1)" : "+v"(fb) : "n"(PL.wPollB) : "memory");
            if (!__all(fb.x == tlo && fb.y == thi))
              wait_flags(rWS, WS_FLAG + (FL_A + ((t64 >> 1) * 14 + (lane < 14 ? lane : 0)) * 2 + 1) * 256, lane < 14, tlo, thi, 8);
          }
        }
        if constexpr (PL.wBar[pos] < 63) VMW(PL.wBar[pos]);
        SBAR();
        if constexpr (pos == 14) { if (tid == 0) set_flag(rWS, FL_A + (tt3 * 14 + fs3) * 2 + 1, tlo, thi); }
      }
    });
    STAMP(21);
    BAR();
    {
      const f32x16 snd = kh ? acc[0] : acc[1];
      const u32 xb = L_XC4 + w * 4096 + lane * 16;
#pragma unroll
      for (int qq = 0; qq < 4; ++qq) { f32x4 v = {snd[4 * qq], snd[4 * qq + 1], snd[4 * qq + 2], snd[4 * qq + 3]}; L.wf4(xb + qq * 1024, v); }
    }
    BAR();
    RELANE();
    {
      const f32x16 mine = kh ? acc[1] : acc[0];
      const u32 xb = L_XC4 + (w ^ 2) * 4096 + lane * 16;
      const u32 row = tt2 * 32 + t32, rsw = (row >> 1) & 7;
      const u32 ob = L_OUT + row * 128 + 8 * h, tg = opq(L_TGB + (kh * 32 + 4 * h) * 4);
      sfor<0, 4>([&](auto qq) {
        constexpr int Q = qq.value;
        const f32x4 o = L.rf4(xb + Q * 1024);
        const f32x4 gb4 = L.rf4(tg + 32 * Q);
        v2u r = {pkv(fma2(lo2(gb4), pr<4 * Q>(mine) + lo2(o), unp(x1p[2 * Q]))), pkv(fma2(hi2(gb4), pr<4 * Q + 2>(mine) + hi2(o), unp(x1p[2 * Q + 1])))};
        L.w64(ob + (((kh * 4 + Q) ^ rsw) * 16), r);
      });
    }
    BAR();
    {
      const u32 rr = lane >> 3, c = lane & 7;
#pragma unroll
      for (int j = 0; j < 2; ++j) {
        const u32 row = w * 16 + 8 * j + rr;
        const v4u v = L.r128(L_OUT + row * 128 + c * 16);
        __builtin_amdgcn_raw_buffer_store_b128(v, rOUT, row * 896 + ((c ^ ((row >> 1) & 7)) * 16), (t64 * 64) * 896 + cs * 128, 0);
      }
    }
    STAMP(22);
    asm volatile(".Lfb_code1:");
  }
}

extern "C" void kernel_launch(void* const* d_in, const int* in_sizes, int n_in, void* d_out, int out_size, void* d_ws, size_t ws_size,
                              hipStream_t stream) {
  (void)in_sizes; (void)n_in; (void)out_size; (void)ws_size;
  (void)hipFuncSetAttribute((const void*)fused_block, hipFuncAttributeMaxDynamicSharedMemorySize, LDS_BYTES);
  fused_block<<<256, 256, LDS_BYTES, stream>>>((const u16*)d_in[0], (const u16*)d_in[1], (const u16*)d_in[2], (const u16*)d_in[3],
                                               (const u16*)d_in[5], (const u16*)d_in[6], (const u16*)d_in[7], (const u16*)d_in[8],
                                               (const u16*)d_in[9], (u16*)d_out, (unsigned char*)d_ws);
}
```

```cpp
#include <hip/hip_runtime.h>
#include <stdint.h>

typedef unsigned u32;
typedef unsigned long long u64;
typedef unsigned short u16;
typedef u32 v4u __attribute__((ext_vector_type(4)));
typedef u32 v2u __attribute__((ext_vector_type(2)));
typedef float f32x2 __attribute__((ext_vector_type(2)));
typedef float f32x4 __attribute__((ext_vector_type(4)));
typedef float f32x16 __attribute__((ext_vector_type(16)));
typedef __bf16 bf16x2 __attribute__((ext_vector_type(2)));
typedef __bf16 bf16x8 __attribute__((ext_vector_type(8)));
typedef short s16x4 __attribute__((ext_vector_type(4)));
typedef __amdgpu_buffer_rsrc_t rsrc_t;
#define LAS __attribute__((address_space(3)))
#define DI __device__ __forceinline__

constexpr u32 WS_EPOCH = 0;
constexpr u32 WS_TMO = 256;
constexpr u32 WS_FLAG = 4096;
constexpr u32 FL_KV = 0, FL_O = 256, FL_X = 512, FL_C = 768, FL_A = 1024;
constexpr u32 WS_MODG = 0x80000;
constexpr u32 MODG_REP = 2688 * 8;
constexpr u32 WS_EXT = 0xB0000;
constexpr u32 WS_STAT = 0xC0000;
constexpr u32 WS_KVP = 0x100000;
constexpr u32 WS_OFR = 0x200000;
constexpr u32 WS_X1F = 0x400000;
constexpr u32 WS_ACT = 0x600000;
constexpr u32 WS_DBG = 0xE00000;
constexpr u32 WS_USED = 0x1000000;

constexpr u32 SLOT = 16384;
constexpr u32 L_XS = 0;
constexpr u32 L_WKV1 = 114688;
constexpr u32 L_WKV2 = 65536;
constexpr u32 L_WQ = 32768;
constexpr u32 L_PHI = 0, L_VFR = 16384;
constexpr u32 L_WO = 65536, L_XCH = 122880, L_XR = 139264;
constexpr u32 L_T1 = 147456;
constexpr u32 L_KVR = 147456;
constexpr u32 L_WPH = 154624;
constexpr u32 L_P0S = 156672;
constexpr u32 L_EXT = 154624;
constexpr u32 L_ST1 = 158720;
constexpr u32 L_TGA = 159232, L_TG2 = 159488, L_TGB = 159744;
constexpr u32 L_CI = 0, L_CT = 147456, L_CB = 149504;
constexpr u32 L_XC4 = 0, L_OUT = 16384;
constexpr u32 L_SIL = 160768;
constexpr u32 L_DUMP = 122880;
constexpr u32 LDS_BYTES = 163840;

constexpr int SPIN_CAP = 40000;
#define NXP 2
#define SPOLL 16

template <int V> struct IC { static constexpr int value = V; };
template <int I, int N, class F> DI void sfor(F&& f) { if constexpr (I < N) { f(IC<I>{}); sfor<I + 1, N>(f); } }

DI rsrc_t mk(const void* p, u32 bytes) { return __builtin_amdgcn_make_buffer_rsrc((void*)p, 0, (int)bytes, 0x00020000); }
DI float bflo(u32 v) { return __uint_as_float(v << 16); }
DI float bfhi(u32 v) { return __uint_as_float(v & 0xffff0000u); }
DI f32x2 unp(u32 v) { f32x2 r = {bflo(v), bfhi(v)}; return r; }
DI u32 pkv(f32x2 v) { return __builtin_bit_cast(u32, __builtin_convertvector(v, bf16x2)); }
DI u32 pk2(float a, float b) { f32x2 v = {a, b}; return pkv(v); }
DI float rbf(float a) { return bflo(pk2(a, a)); }
DI f32x2 bc(float a) { f32x2 r = {a, a}; return r; }
DI f32x2 fma2(f32x2 a, f32x2 b, f32x2 c) { return __builtin_elementwise_fma(a, b, c); }
DI f32x2 lo2(f32x4 v) { f32x2 r = {v[0], v[1]}; return r; }
DI f32x2 hi2(f32x4 v) { f32x2 r = {v[2], v[3]}; return r; }
template <int R> DI f32x2 pr(const f32x16& a) { f32x2 r = {a[R], a[R + 1]}; return r; }
DI float dot2(u32 a, u32 b, float c) { return __builtin_amdgcn_fdot2_f32_bf16(__builtin_bit_cast(bf16x2, a), __builtin_bit_cast(bf16x2, b), c, false); }
DI bf16x8 asb(v4u v) { return __builtin_bit_cast(bf16x8, v); }
DI f32x16 mfma(v4u a, v4u b, f32x16 c) { return __builtin_amdgcn_mfma_f32_32x32x16_bf16(asb(a), asb(b), c, 0, 0, 0); }
DI float half_sum(float v) { auto s = __builtin_amdgcn_permlane32_swap(__float_as_uint(v), __float_as_uint(v), false, false); return __uint_as_float(s[0]) + __uint_as_float(s[1]); }
DI float half_max(float v) { auto s = __builtin_amdgcn_permlane32_swap(__float_as_uint(v), __float_as_uint(v), false, false); return fmaxf(__uint_as_float(s[0]), __uint_as_float(s[1])); }
DI float half_min(float v) { auto s = __builtin_amdgcn_permlane32_swap(__float_as_uint(v), __float_as_uint(v), false, false); return fminf(__uint_as_float(s[0]), __uint_as_float(s[1])); }
DI float half_hi(float v) { auto s = __builtin_amdgcn_permlane32_swap(__float_as_uint(v), __float_as_uint(v), false, false); return __uint_as_float(s[1]); }
template <int CTRL> DI float dpp_add(float v) { return v + __int_as_float(__builtin_amdgcn_update_dpp(0, __float_as_int(v), CTRL, 0xf, 0xf, true)); }
#define ROR2 0x122
#define ROR4 0x124
#define ROR8 0x128
#define BAR() asm volatile("s_waitcnt lgkmcnt(0)\n\ts_barrier" ::: "memory")
#define VMW(n) asm volatile("s_waitcnt vmcnt(%0)" ::"n"(n) : "memory")
#define SCHED() __builtin_amdgcn_sched_barrier(0)

template <int N, int L0, int LS, int G0, int GS> DI void dma(u32 voff, rsrc_t rs, u32 sg, u32 sl) {
  u32 t;
  asm volatile("s_add_u32 m0, %3, %5\n\ts_add_u32 %0, %2, %6\n\tbuffer_load_dwordx4 %1, %4, %0 offen lds\n\t"
               ".rept %7\n\ts_add_u32 m0, m0, %8\n\ts_add_u32 %0, %0, %9\n\tbuffer_load_dwordx4 %1, %4, %0 offen lds\n\t.endr"
               : "=&s"(t) : "v"(voff), "s"(sg), "s"(sl), "s"(rs), "n"(L0), "n"(G0), "n"(N - 1), "n"(LS), "n"(GS) : "memory", "scc");
}
template <int R> DI void dma_x4(u32 voff, rsrc_t rs, u32 sg, u32 sl) {
  u32 t;
  asm volatile("s_add_u32 m0, %3, %5\n\ts_add_u32 %0, %2, %5\n\tbuffer_load_dwordx4 %1, %4, %0 offen lds\n\t"
               "s_add_u32 m0, m0, 896\n\ts_add_u32 %0, %0, 896\n\tbuffer_load_dwordx4 %1, %4, %0 offen lds\n\t"
               "s_add_u32 m0, m0, 13440\n\ts_add_u32 %0, %0, 13440\n\tbuffer_load_dwordx4 %1, %4, %0 offen lds\n\t"
               "s_add_u32 m0, m0, 896\n\ts_add_u32 %0, %0, 896\n\tbuffer_load_dwordx4 %1, %4, %0 offen lds"
               : "=&s"(t) : "v"(voff), "s"(sg), "s"(sl), "s"(rs), "n"(R * 896) : "memory", "scc");
}
DI void warm(u32 voff, rsrc_t rs, u32 sg, u32 sl) {
  asm volatile("s_add_u32 m0, %3, %4\n\ts_nop 0\n\tbuffer_load_dword %0, %1, %2 offen lds" ::"v"(voff), "s"(rs), "s"(sg), "s"(sl), "n"(L_DUMP) : "memory", "scc");
}
template <int L0, int G0> DI void dma3(u32 v0, u32 v1, u32 v2, rsrc_t rs, u32 sg, u32 sl) {
  u32 t;
  asm volatile("s_add_u32 m0, %5, %7\n\ts_add_u32 %0, %4, %8\n\tbuffer_load_dwordx4 %1, %6, %0 offen lds\n\t"
               "s_add_u32 m0, m0, 2048\n\ts_nop 0\n\tbuffer_load_dwordx4 %2, %6, %0 offen lds\n\t"
               "s_add_u32 m0, m0, 2048\n\ts_nop 0\n\tbuffer_load_dwordx4 %3, %6, %0 offen lds"
               : "=&s"(t) : "v"(v0), "v"(v1), "v"(v2), "s"(sg), "s"(sl), "s"(rs), "n"(L0), "n"(G0) : "memory", "scc");
}
template <int IMM> DI void ald128(v4u& d, u32 voff, rsrc_t rs, u32 soff) {
  asm volatile("buffer_load_dwordx4 %0, %1, %2, %3 offen offset:%4" : "=v"(d) : "v"(voff), "s"(rs), "s"(soff), "n"(IMM) : "memory");
}
template <int IMM> DI void ald128c(v4u& d, u32 voff, rsrc_t rs, u32 soff) {
  asm volatile("buffer_load_dwordx4 %0, %1, %2, %3 offen offset:%4 sc1" : "=v"(d) : "v"(voff), "s"(rs), "s"(soff), "n"(IMM) : "memory");
}
DI void ald32(u32& d, u32 voff, rsrc_t rs, u32 soff) { asm volatile("buffer_load_dword %0, %1, %2, %3 offen" : "=v"(d) : "v"(voff), "s"(rs), "s"(soff) : "memory"); }
DI void ald64(v2u& d, u32 voff, rsrc_t rs, u32 soff) { asm volatile("buffer_load_dwordx2 %0, %1, %2, %3 offen" : "=v"(d) : "v"(voff), "s"(rs), "s"(soff) : "memory"); }
DI void ald64c(v2u& d, u32 voff, rsrc_t rs, u32 soff) { asm volatile("buffer_load_dwordx2 %0, %1, %2, %3 offen sc1" : "=v"(d) : "v"(voff), "s"(rs), "s"(soff) : "memory"); }
#define TIE(x) asm volatile("" : "+v"(x))

DI v4u ld128(rsrc_t r, u32 off, u32 so = 0) { return __builtin_amdgcn_raw_buffer_load_b128(r, off, so, 0); }
DI v4u ld128c(rsrc_t r, u32 off, u32 so = 0) { return __builtin_amdgcn_raw_buffer_load_b128(r, off, so, 16); }
DI v2u ld64(rsrc_t r, u32 off, u32 so = 0) { return __builtin_amdgcn_raw_buffer_load_b64(r, off, so, 0); }
DI v2u ld64c(rsrc_t r, u32 off, u32 so = 0) { return __builtin_amdgcn_raw_buffer_load_b64(r, off, so, 16); }
DI void st128c(rsrc_t r, u32 off, u32 so, v4u v) { __builtin_amdgcn_raw_buffer_store_b128(v, r, off, so, 16); }
DI void st64c(rsrc_t r, u32 off, u32 so, v2u v) { __builtin_amdgcn_raw_buffer_store_b64(v, r, off, so, 16); }
DI void st32c(rsrc_t r, u32 off, u32 so, u32 v) { __builtin_amdgcn_raw_buffer_store_b32(v, r, off, so, 16); }

struct Lds {
  LAS unsigned char* p;
  DI v4u r128(u32 o) const { return *(LAS v4u*)(p + o); }
  DI v2u r64(u32 o) const { return *(LAS v2u*)(p + o); }
  DI f32x4 rf4(u32 o) const { return *(LAS f32x4*)(p + o); }
  DI f32x2 rf2(u32 o) const { return *(LAS f32x2*)(p + o); }
  DI float rf(u32 o) const { return *(LAS float*)(p + o); }
  DI void w128(u32 o, v4u v) const { *(LAS v4u*)(p + o) = v; }
  DI void w64(u32 o, v2u v) const { *(LAS v2u*)(p + o) = v; }
  DI void wf4(u32 o, f32x4 v) const { *(LAS f32x4*)(p + o) = v; }
  DI void wf2(u32 o, f32x2 v) const { *(LAS f32x2*)(p + o) = v; }
  DI void wf(u32 o, float v) const { *(LAS float*)(p + o) = v; }
  DI v2u tr(u32 o) const { return __builtin_bit_cast(v2u, __builtin_amdgcn_ds_read_tr16_b64_v4i16((LAS s16x4*)(p + o))); }
  DI v4u tr2(u32 o0, u32 o1) const { v2u a = tr(o0), b = tr(o1); v4u r = {a.x, a.y, b.x, b.y}; return r; }
};

DI void wait_flags(rsrc_t ws, u32 off, bool active, u32 tlo, u32 thi, u32 code) {
  for (int spins = 0;; ++spins) {
    asm volatile("" ::: "memory");
    v2u v = ld64c(ws, off);
    bool ok = !active || (v.x == tlo && v.y == thi);
    if (__all(ok)) break;
    if (spins > SPIN_CAP) { st32c(ws, WS_TMO, 0, code); break; }
    __builtin_amdgcn_s_sleep(1);
  }
  asm volatile("" ::: "memory");
}
DI void set_flag(rsrc_t ws, u32 idx, u32 tlo, u32 thi) { v2u t = {tlo, thi}; st64c(ws, WS_FLAG + idx * 256, 0, t); }
DI bool gran_ok(v4u a, u32 glo, u32 ghi) { return (a.x & 0xffff0000u) == glo && a.y == ghi && (a.z & 0xffff0000u) == glo && a.w == ghi; }

#define STAMP(i)

DI void feature_map(const f32x16& pin, int h, u32 (&P1)[8], u32 (&P2)[8]) {
  const float L2E = 1.4426950408889634f, INF = __builtin_inff();
  float pm[16], pn[16];
#pragma unroll
  for (int r = 0; r < 16; ++r) { pm[r] = pin[r]; pn[r] = pin[r]; }
#pragma unroll
  for (int r = 12; r < 16; ++r) { pm[r] = h ? -INF : pin[r]; pn[r] = h ? INF : pin[r]; }
  float mx = fmaxf(pm[0], pm[1]), mn = fminf(pn[0], pn[1]);
#pragma unroll
  for (int r = 2; r < 16; r += 2) { mx = fmaxf(fmaxf(mx, pm[r]), pm[r + 1]); mn = fminf(fminf(mn, pn[r]), pn[r + 1]); }
  mx = half_max(mx); mn = half_min(mn);
  const f32x2 cmx = bc(-mx * L2E), cmn = bc(mn * L2E), pl = bc(L2E), nl = bc(-L2E);
  f32x2 e1[8], e2[8];
#pragma unroll
  for (int i = 0; i < 8; ++i) {
    const f32x2 a = {pm[2 * i], pm[2 * i + 1]}, b = {pn[2 * i], pn[2 * i + 1]};
    const f32x2 ta = fma2(a, pl, cmx), tb = fma2(b, nl, cmn);
    e1[i] = f32x2{__builtin_amdgcn_exp2f(ta.x), __builtin_amdgcn_exp2f(ta.y)};
    e2[i] = f32x2{__builtin_amdgcn_exp2f(tb.x), __builtin_amdgcn_exp2f(tb.y)};
  }
  f32x2 s1 = ((e1[0] + e1[1]) + (e1[2] + e1[3])) + ((e1[4] + e1[5]) + (e1[6] + e1[7]));
  f32x2 s2 = ((e2[0] + e2[1]) + (e2[2] + e2[3])) + ((e2[4] + e2[5]) + (e2[6] + e2[7]));
  const f32x2 r1 = bc(__builtin_amdgcn_rcpf(half_sum(s1.x + s1.y))), r2 = bc(__builtin_amdgcn_rcpf(half_sum(s2.x + s2.y)));
#pragma unroll
  for (int i = 0; i < 8; ++i) { P1[i] = pkv(e1[i] * r1); P2[i] = pkv(e2[i] * r2); }
}

constexpr int NB0 = 12;
constexpr int SST = 8;
constexpr int AW = 12;
constexpr int PA_I = 12, PA_C = 15;
constexpr int PB_I = 10, PB_C = 15;
struct Plan { int wS, wB[28], wBar[28], wPollA, wAct[56], wPollB; };
constexpr int clampv(int v) { return v > 63 ? 63 : v; }
constexpr Plan mk_plan() {
  Plan P{};
  int n = 0, iPiece[30] = {}, iB[28] = {}, iA[56] = {}, iStore = 0, iPoll = 0;
  for (int i = 0; i < 30; ++i) iPiece[i] = -1000;
  n += 1;
  n += 7; const int iS = n - 1;
  for (int s = 0; s < NB0; ++s) iB[s] = n++;
  for (int G = 0; G < 2; ++G) {
    for (int s = 0; s < 28; ++s) {
      const int pos = G * 7 + s / 4;
      if (G == 0) P.wB[s] = clampv(n - 1 - iB[s]);
      if (G == 0 && s == SST) P.wS = clampv(n - 1 - iS);
      if (G == 1 && s == PA_I) iPoll = n - 1;
      if (pos >= 1) iPiece[pos + 8] = n++;
      if (G == 0 && s + NB0 < 28) iB[s + NB0] = n++;
      if (G == 1 && s > PA_C) iA[s - PA_C - 1] = n++;
      if ((s & 3) == 3) {
        int wv = n - 1 - iPiece[pos + 2];
        if (pos == 8 && n - 1 - iStore < wv) wv = n - 1 - iStore;
        P.wBar[pos] = clampv(wv);
      }
      if (G == 1 && s == PA_C) P.wPollA = clampv(n - 1 - iPoll);
    }
    n += 4; iStore = n - 1;
  }
  for (int o = 0; o < 56; ++o) {
    const int pos = 14 + o / 4;
    if (o == PB_I) iPoll = n - 1;
    P.wAct[o] = clampv(n - 1 - iA[o]);
    if (o < 24) iPiece[pos + 8] = n++;
    if (o + AW < 56) iA[o + AW] = n++;
    if ((o & 3) == 3) {
      int wv = pos + 2 <= 27 ? n - 1 - iPiece[pos + 2] : 63;
      if (pos == 14 && n - 1 - iStore < wv) wv = n - 1 - iStore;
      P.wBar[pos] = clampv(wv);
    }
    if (o == PB_C) P.wPollB = clampv(n - 1 - iPoll);
  }
  return P;
}
constexpr Plan PL = mk_plan();
static_assert(27 - PA_C == AW && (PA_C & 3) == 3 && (PB_C & 3) == 3 && PB_C + 1 + AW == 28, "plan");

DI float silu_f(float c) { return c * __builtin_amdgcn_rcpf(1.f + __builtin_amdgcn_exp2f(-1.4426950408889634f * c)); }
DI u32 cmask(u32 v, u64 m) { u32 r; asm("v_cndmask_b32_e64 %0, 0, %1, %2" : "=v"(r) : "v"(v), "s"(m)); return r; }
DI u32 opq(u32 v) { asm("" : "+v"(v)); return v; }
DI void split2(float v, u32& hi, u32& lo) { hi = pk2(v, v) & 0xffffu; lo = pk2(v - bflo(hi), 0.f) & 0xffffu; }
#define SBAR() asm volatile("s_barrier" ::: "memory")

__global__ void __launch_bounds__(256, 2)
fused_block(const u16* __restrict__ gx, const u16* __restrict__ gwqkv, const u16* __restrict__ gwphi, const u16* __restrict__ gwo,
            const u16* __restrict__ gwg, const u16* __restrict__ gwu, const u16* __restrict__ gwd, const u16* __restrict__ gc,
            const u16* __restrict__ gwmod, u16* __restrict__ gout, unsigned char* __restrict__ gws) {
  extern __shared__ __attribute__((aligned(16))) unsigned char smem[];
  asm volatile("" ::"s"(gx), "s"(gwqkv), "s"(gwphi), "s"(gwo), "s"(gwg), "s"(gwu), "s"(gwd), "s"(gc), "s"(gwmod), "s"(gout), "s"(gws));
  Lds L; L.p = (LAS unsigned char*)smem;
  const u32 lds0 = (u32)(uintptr_t)L.p;
  int tid = threadIdx.x, lane = tid & 63;
  const int w = __builtin_amdgcn_readfirstlane(tid >> 6);
  const int bid = blockIdx.x, xq = bid & 7, iq = bid >> 3;
  int h, t32, g16, q4, p4;
#define RELANE() do { asm volatile("" : "+v"(tid)); lane = tid & 63; h = lane >> 5; t32 = lane & 31; g16 = (lane >> 4) & 1; q4 = (lane & 15) >> 2; p4 = lane & 3; } while (0)
  RELANE();

  const rsrc_t rWS = mk(gws, WS_USED);
  const rsrc_t rX = mk(gx, 2048 * 896), rWQKV = mk(gwqkv, 448 * 2688), rWPHI = mk(gwphi, 16 * 784 * 2), rWO = mk(gwo, 448 * 896);
  const rsrc_t rWG = mk(gwg, 448 * 3584), rWU = mk(gwu, 448 * 3584), rWD = mk(gwd, 1792 * 896), rC = mk(gc, 896), rWM = mk(gwmod, 448 * 5376);
  const rsrc_t rOUT = mk(gout, 2048 * 896);

  const int hd = 8 * (xq & 1) + (iq & 7), tt = 4 * (xq >> 1) + (iq >> 3);
  const bool p24 = iq < 28;
  const int t64 = 4 * xq + iq / 7, cs = iq % 7;
  const int tt3 = 4 * (xq >> 1) + iq / 7, fs3 = 7 * (xq & 1) + cs;
  const u32 gmask = (xq & 1) ? 0xD5D5D5D5u : 0xABABABABu;
  const int rank = ((gmask >> iq) & 1u) ? __builtin_popcount(gmask & ((1u << iq) - 1u)) : 99;
  const int chunkC = (rank >> 1) * 16 + 2 * xq + (rank & 1), chunkN = chunkC - 4;
  const bool p0 = rank < 4 && chunkC < 28, p0n = rank >= 4 && rank < 12 && chunkN < 84;
  const u32 zero = 0;

  u32 cpf = 0;
  {
    u64 pc; u32 clen;
    asm volatile("s_getpc_b64 %0\n.Lfb_code0:\n\ts_mov_b32 %1, .Lfb_code1-.Lfb_code0" : "=s"(pc), "=s"(clen));
    if (w == 0) {
      const u32 o = min((u32)(iq * 32 + (lane & 31)) * 64u, clen - 4u);
      asm volatile("global_load_dword %0, %1, %2" : "=v"(cpf) : "v"(o), "s"(pc) : "memory");
    }
    if (w == 3) ald32(cpf, min((u32)(bid * 37 + min(lane, 36)), 9407u) * 128u, rWQKV, 0u);
  }
  STAMP(0);
  v2u ep;
  ald64c(ep, zero, rWS, zero);
  u32 tlo, thi, glo, ghi;
  auto get_tag = [&]() {
    const u64 E = ((u64)__builtin_amdgcn_readfirstlane(ep.y) << 32) | (u32)__builtin_amdgcn_readfirstlane(ep.x);
    const u64 tag = E + 1;
    tlo = (u32)tag; thi = (u32)(tag >> 32);
    glo = tlo << 16; ghi = (u32)(tag >> 16);
  };
  auto x_dma = [&](auto k0, auto k1) {
    if (lane < 56) {
      const u32 sg = (tt * 128 + w * 32) * 896, sl = lds0 + L_XS + w * 28672;
      sfor<k0.value, k1.value>([&](auto k) { dma_x4<2 * k.value>((lane * 16) ^ (k.value * 16), rX, sg, sl); });
    }
  };
  auto silu2 = [&](u32 c2) -> u32 { return pk2(silu_f(bflo(c2)), silu_f(bfhi(c2))); };
  auto p0_finish = [&](const v4u& sp, const v4u (&wm)[8], int chunk) {
    float acc[8] = {};
#pragma unroll
    for (int jp = 0; jp < 4; ++jp)
#pragma unroll
      for (int e = 0; e < 4; ++e) {
        acc[2 * e] = dot2(__builtin_amdgcn_perm(wm[2 * jp + 1][e], wm[2 * jp][e], 0x05040100u), sp[jp], acc[2 * e]);
        acc[2 * e + 1] = dot2(__builtin_amdgcn_perm(wm[2 * jp + 1][e], wm[2 * jp][e], 0x07060302u), sp[jp], acc[2 * e + 1]);
      }
    float r[8];
#pragma unroll
    for (int j = 0; j < 8; ++j) r[j] = dpp_add<ROR8>(dpp_add<ROR4>(acc[j]));
    if ((lane & 12) == 0) {
      const u32 base = L_P0S + (w * 4 + (lane >> 4)) * 128 + (lane & 3) * 32;
      f32x4 a = {r[0], r[1], r[2], r[3]}, b = {r[4], r[5], r[6], r[7]};
      L.wf4(base, a); L.wf4(base + 16, b);
    }
    BAR();
    if (w < 2) {
      const int rep = w * 4 + (lane >> 4), pr_ = lane & 15;
      f32x2 v = {0.f, 0.f};
#pragma unroll
      for (int gI = 0; gI < 16; ++gI) v += L.rf2(L_P0S + gI * 128 + pr_ * 8);
      float v0 = rbf(v.x), v1 = rbf(v.y);
      if ((chunk >= 14 && chunk < 28) || (chunk >= 56 && chunk < 70)) { v0 = 1.f + v0; v1 = 1.f + v1; }
      const u32 pk = pk2(v0, v1);
      v4u gr = {glo | (pk & 0xffffu), ghi, glo | (pk >> 16), ghi};
      st128c(rWS, rep * MODG_REP + 2 * pr_ * 8, WS_MODG + chunk * 256, gr);
    }
  };
  if (p0) {
    v4u cw, wm[8];
    const u32 r8 = (tid >> 2) * 8;
    ald128<0>(cw, r8 * 2, rC, zero);
    const u32 vo = r8 * 5376 + chunkC * 64 + (tid & 3) * 16;
    sfor<0, 8>([&](auto j) { ald128<0>(wm[j.value], vo, rWM, j.value * 5376); });
    x_dma(IC<0>{}, IC<NXP>{});
    asm volatile("s_waitcnt vmcnt(%2)" : "+v"(ep), "+v"(cw) : "n"(8 + 4 * NXP) : "memory");
    get_tag();
    const v4u sp = {silu2(cw.x), silu2(cw.y), silu2(cw.z), silu2(cw.w)};
    asm volatile("s_waitcnt vmcnt(%8)" : "+v"(wm[0]), "+v"(wm[1]), "+v"(wm[2]), "+v"(wm[3]), "+v"(wm[4]), "+v"(wm[5]), "+v"(wm[6]), "+v"(wm[7]) : "n"(4 * NXP) : "memory");
    p0_finish(sp, wm, chunkC);
    STAMP(31);
    x_dma(IC<NXP>{}, IC<8>{});
  } else {
    x_dma(IC<0>{}, IC<8>{});
  }
  STAMP(1);
  const u32 vW = (lane >> 2) * 2688 + hd * 56 + min((lane & 3) * 16, 40);
  const u32 sgW = w * 43008;
  v2u wph;
  ald64(wph, ((tid >> 3) < 28 && (tid & 7) < 7) ? (hd * 784 + (tid >> 3) * 28 + (tid & 7) * 4) * 2 : 0x7ffffff0, rWPHI, zero);
  u32 c2;
  ald32(c2, tid < 224 ? tid * 4 : 0x7ffffff0, rC, zero);
  v4u ga_, gb_;
  const u32 so_mod = WS_MODG + xq * MODG_REP;

  if (NXP == 8 && p0 && w < 2) VMW(3); else VMW(2);
  asm volatile("" : "+v"(ep), "+v"(cpf)::"memory");
  if (!p0) get_tag();
  v4u X[28];
  {
    const u32 gsw = (t32 >> 1) & 7;
    u32 xb[4];
#pragma unroll
    for (int j = 0; j < 4; ++j) xb[j] = L_XS + w * 28672 + t32 * 896 + (((2 * j + h) ^ gsw) * 16);
#pragma unroll
    for (int s = 0; s < 28; ++s) X[s] = s < 3 ? L.r128(xb[s & 3] + (s >> 2) * 128) : v4u{};
    sfor<0, 8>([&](auto pc) {
      constexpr int n = pc.value >> 1, m = pc.value & 1;
      dma<1, L_WKV1 + n * 8192 + m * 1024, 0, n * 4 * 43008 + 896 + m * 896, 0>(vW, rWQKV, sgW, lds0 + w * 2048);
#pragma unroll
      for (int s = 3 * pc.value + 3; s < 3 * pc.value + 6; ++s) X[s] = L.r128(xb[s & 3] + (s >> 2) * 128);
    });
    X[27] = L.r128(xb[3] + 6 * 128);
  }
  BAR();
  STAMP(2);
  float rstd1;
  v4u Bc;
  {
    float s0 = 0.f, s1 = 0.f, q0 = 0.f, q1 = 0.f;
    sfor<0, 28>([&](auto sc) {
      constexpr int s = sc.value;
      if constexpr (s == SPOLL) {
        ald128c<0>(ga_, tid * 16, rWS, so_mod);
        ald128c<448 * 8>(gb_, tid * 16, rWS, so_mod);
      }
      if constexpr (s % 4 == 0 && s < 24) {
        constexpr int n = 4 + s / 8, m = (s / 4) & 1;
        dma<1, L_WKV2 + (n - 4) * 8192 + m * 1024, 0, n * 4 * 43008 + 896 + m * 896, 0>(vW, rWQKV, sgW, lds0 + w * 2048);
        SCHED();
      }
      s0 = dot2(X[s][0], 0x3f803f80u, s0); s1 = dot2(X[s][1], 0x3f803f80u, s1);
      s0 = dot2(X[s][2], 0x3f803f80u, s0); s1 = dot2(X[s][3], 0x3f803f80u, s1);
      q0 = dot2(X[s][0], X[s][0], q0); q1 = dot2(X[s][1], X[s][1], q1);
      q0 = dot2(X[s][2], X[s][2], q0); q1 = dot2(X[s][3], X[s][3], q1);
      if constexpr (s % 4 == 3) SCHED();
    });
    const float m = half_sum(s0 + s1) * (1.f / 448.f);
    const float var = fmaxf(half_sum(q0 + q1) * (1.f / 448.f) - m * m, 0.f) + 1e-5f;
    rstd1 = __builtin_amdgcn_rsqf(var);
    u32 mh, ml, sh, sl_;
    split2(-m, mh, ml); split2(var * rstd1, sh, sl_);
    Bc = h ? v4u{0u, 0u, 0u, 0u} : v4u{mh | (mh << 16), ml | (ml << 16), sh | (sl_ << 16), 0u};
    if (h == 0) L.wf(L_ST1 + (w * 32 + t32) * 4, rstd1);
  }
  asm volatile("s_waitcnt vmcnt(%4)" : "+v"(wph), "+v"(ga_), "+v"(gb_), "+v"(c2) : "n"(SPOLL >= 21 ? 0 : 6 - (SPOLL + 3) / 4) : "memory");
  L.w64(L_WPH + (tid >> 3) * 64 + (tid & 7) * 8, wph);
  if (p0n) *(LAS u32*)(L.p + L_SIL + tid * 4) = silu2(c2);
  STAMP(3);
  {
    const bool act = tid < 224;
    for (int spins = 0;; ++spins) {
      const bool ok = !act || (gran_ok(ga_, glo, ghi) && gran_ok(gb_, glo, ghi));
      if (__all(ok)) break;
      if (spins > SPIN_CAP) { st32c(rWS, WS_TMO, 0, 1); break; }
      asm volatile("" ::: "memory");
      ga_ = ld128c(rWS, tid * 16, so_mod); gb_ = ld128c(rWS, tid * 16 + 448 * 8, so_mod);
    }
    if (act) {
      const u32 g0 = gb_.x & 0xffffu, g1 = gb_.z << 16, s0 = (ga_.x & 0xffffu) * 0x10001u, s1 = (ga_.z & 0xffffu) * 0x10001u;
      L.w128(L_T1 + tid * 32, v4u{g0, g0, s0, s0}); L.w128(L_T1 + tid * 32 + 16, v4u{g1, g1, s1, s1});
    }
  }
  u64 m1[4], m2[4];
#pragma unroll
  for (int d = 0; d < 4; ++d) {
    m1[d] = (3ull << (2 * d)) | (3ull << (40 + 2 * d)); m2[d] = (3ull << (16 + 2 * d)) | (3ull << (56 + 2 * d));
    asm volatile("" : "+s"(m1[d]), "+s"(m2[d]));
  }
  RELANE();
  const u32 coff = g16 * 32 + ((p4 ^ (g16 & (p4 >> 1))) * 8);
  const u32 tb = opq(L_T1 + t32 * 16);
  const u32 wb1 = opq(L_WKV1 + (4 * h + q4) * 64 + coff), wb2 = opq(L_WKV2 + (4 * h + q4) * 64 + coff);
  BAR();
  STAMP(4);

  const rsrc_t rPF0 = w == 2 ? rWG : rWD, rPF1 = w == 2 ? rWU : rWO;
  const u32 vPF0 = w >= 2 ? (bid * 49 + min(lane, 48)) * 128 : 0x7ffffff0;
  const u32 vPF1 = w == 2 ? vPF0 : (w == 3 ? min(bid * 13 + min(lane, 12), 3135) * 128 : 0x7ffffff0);
  const u32 vPM = (p0n && w >= 2) ? ((rank & 1) * 224 + (w - 2) * 112 + min(lane, 55)) * 5376 + chunkN * 64 : 0x7ffffff0;
  f32x16 accK = {}, accV = {};
  v4u H[28];
  {
    auto rdW = [&](int s, int mat) -> v4u { const u32 b = s < 16 ? wb1 + s * 2048 + mat * 1024 : wb2 + (s - 16) * 2048 + mat * 1024; return L.tr2(b, b + 512); };
    auto mkA = [&](const v4u& ac, const u64 (&m)[4]) -> v4u { v4u a = {cmask(ac.x, m[0]), cmask(ac.x, m[1]), cmask(ac.x, m[2]), cmask(ac.x, m[3])}; return a; };
    const f32x16 Z = {};
    f32x16 D[2];
    v4u Ac[2], A1, A2, fk[2], fv[2];
    Ac[0] = L.r128(tb); Ac[1] = L.r128(tb + 512);
    const v4u Ac2 = L.r128(tb + 2 * 512);
    fk[0] = rdW(0, 0); fv[0] = rdW(0, 1); fk[1] = rdW(1, 0); fv[1] = rdW(1, 1);
    SCHED();
    D[0] = mfma(mkA(Ac[0], m1), X[0], Z);
    D[1] = mfma(mkA(Ac[1], m1), X[2], Z);
    D[0] = mfma(mkA(Ac[0], m2), X[1], D[0]);
    D[1] = mfma(mkA(Ac[1], m2), X[3], D[1]);
    D[0] = mfma(Ac[0], Bc, D[0]);
    D[1] = mfma(Ac[1], Bc, D[1]);
    Ac[0] = Ac2;
    A1 = mkA(Ac[0], m1);
    H[0] = v4u{pkv(pr<0>(D[0])), pkv(pr<2>(D[0])), pkv(pr<4>(D[0])), pkv(pr<6>(D[0]))};
    H[1] = v4u{pkv(pr<8>(D[0])), pkv(pr<10>(D[0])), pkv(pr<12>(D[0])), pkv(pr<14>(D[0]))};
    SCHED();
    sfor<0, 14>([&](auto pc_) {
      constexpr int p = pc_.value;
      constexpr bool nd = p + 2 < 14, nc = p + 1 < 14, n3 = p + 3 < 14;
      if constexpr (p == 0) STAMP(28);
      if constexpr (p == 7) { STAMP(29); VMW(7); SBAR(); STAMP(30); }
      f32x16& Dn = D[p & 1];
      const f32x16& Dc = D[(p + 1) & 1];
      if constexpr (nd) { Dn = mfma(A1, X[2 * p + 4], Z); SCHED(); A2 = mkA(Ac[p & 1], m2); }
      SCHED();
      accK = mfma(fk[0], H[2 * p], accK);
      SCHED();
      if constexpr (nc) { fk[0] = rdW(2 * p + 2, 0); H[2 * p + 2].x = pkv(pr<0>(Dc)); H[2 * p + 2].y = pkv(pr<2>(Dc)); }
      SCHED();
      if constexpr (nd) Dn = mfma(A2, X[2 * p + 5], Dn);
      SCHED();
      if constexpr (n3) Ac[(p + 1) & 1] = L.r128(tb + (p + 3) * 512);
      if constexpr (nc) { H[2 * p + 2].z = pkv(pr<4>(Dc)); H[2 * p + 2].w = pkv(pr<6>(Dc)); H[2 * p + 3].x = pkv(pr<8>(Dc)); }
      SCHED();
      accV = mfma(H[2 * p], fv[0], accV);
      SCHED();
      if constexpr (nc) { fv[0] = rdW(2 * p + 2, 1); H[2 * p + 3].y = pkv(pr<10>(Dc)); H[2 * p + 3].z = pkv(pr<12>(Dc)); }
      SCHED();
      if constexpr (nd) Dn = mfma(Ac[p & 1], Bc, Dn);
      SCHED();
      if constexpr (n3) A1 = mkA(Ac[(p + 1) & 1], m1);
      SCHED();
      accK = mfma(fk[1], H[2 * p + 1], accK);
      SCHED();
      if constexpr (nc) { fk[1] = rdW(2 * p + 3, 0); H[2 * p + 3].w = pkv(pr<14>(Dc)); }
      if constexpr (p < 7) dma<1, L_WQ + p * 4096, 0, p * 4 * 43008, 0>(vW, rWQKV, sgW, lds0 + w * 1024);
      else if constexpr (p == 7) warm(vPF0, rPF0, zero, lds0 + w * 1024);
      else if constexpr (p == 8) warm(vPF1, rPF1, zero, lds0 + w * 1024);
      else if constexpr (p < 11) warm(vPM, rWM, (p - 9) * 56 * 5376, lds0 + w * 1024);
      SCHED();
      accV = mfma(H[2 * p + 1], fv[1], accV);
      SCHED();
      if constexpr (nc) fv[1] = rdW(2 * p + 3, 1);
      SCHED();
    });
  }
  STAMP(5);
  v4u PH[2];
  {
    const u32 b = opq(L_WPH + (4 * h + q4) * 64 + g16 * 32 + p4 * 8);
    PH[0] = L.tr2(b, b + 8 * 64); PH[1] = L.tr2(b + 16 * 64, b + 24 * 64);
  }
  auto epi_t = [&](const f32x16& a, v4u& b0, v4u& b1) {
    const f32x2 r = bc(rstd1);
    b0 = v4u{pkv(pr<0>(a) * r), pkv(pr<2>(a) * r), pkv(pr<4>(a) * r), pkv(pr<6>(a) * r)};
    b1 = v4u{pkv(pr<8>(a) * r), pkv(pr<10>(a) * r), pkv(pr<12>(a) * r), pkv(pr<14>(a) * r)};
  };
  {
    v4u b0, b1;
    epi_t(accK, b0, b1);
    f32x16 pk = {};
    pk = mfma(PH[0], b0, pk); pk = mfma(PH[1], b1, pk);
    {
      const float one = t32 == 28 ? 1.f : 0.f;
      const u32 sb_ = opq(L_ST1 + (w * 32 + 4 * h) * 4);
      u32 vb[8];
#pragma unroll
      for (int qq = 0; qq < 4; ++qq) {
        const f32x4 r4 = L.rf4(sb_ + 32 * qq);
        float v[4];
#pragma unroll
        for (int e = 0; e < 4; ++e) { v[e] = accV[4 * qq + e] * r4[e]; v[e] = t32 < 28 ? v[e] : one; }
        vb[2 * qq] = pk2(v[0], v[1]); vb[2 * qq + 1] = pk2(v[2], v[3]);
      }
      v4u a = {vb[0], vb[1], vb[2], vb[3]}, b = {vb[4], vb[5], vb[6], vb[7]};
      L.w128(L_VFR + w * 2048 + lane * 16, a); L.w128(L_VFR + w * 2048 + 1024 + lane * 16, b);
    }
    u32 P1[8], P2[8];
    feature_map(pk, h, P1, P2);
    const u32 swt = (t32 & 7) | ((((t32 >> 3) ^ (t32 >> 1)) & 1) << 3);
    const u32 rb = L_PHI + w * 4096 + t32 * 128;
#pragma unroll
    for (int qq = 0; qq < 4; ++qq) {
      v2u a = {P1[2 * qq], P1[2 * qq + 1]}, b = {P2[2 * qq], P2[2 * qq + 1]};
      L.w64(rb + (((2 * qq + h) ^ swt) * 8), a);
      L.w64(rb + (((8 + 2 * qq + h) ^ swt) * 8), b);
    }
  }
  VMW(4);
  BAR();
  STAMP(6);
  if (w < 2) {
    f32x16 kv = {};
    u32 ad[4];
#pragma unroll
    for (int su = 0; su < 4; ++su) {
      const u32 t = 8 * su + 4 * h + q4;
      const u32 sw_ = (t & 7) | ((((t >> 3) ^ (t >> 1)) & 1) << 3);
      ad[su] = L_PHI + t * 128 + (((8 * w + 4 * g16 + p4) ^ sw_) * 8);
    }
#pragma unroll
    for (int ww = 0; ww < 4; ++ww)
#pragma unroll
      for (int s = 0; s < 2; ++s) {
        const v4u a = L.tr2(ad[2 * s] + ww * 4096, ad[2 * s + 1] + ww * 4096);
        const v4u b = L.r128(L_VFR + ww * 2048 + s * 1024 + lane * 16);
        kv = mfma(a, b, kv);
      }
    v4u o0 = {pkv(pr<0>(kv)), pkv(pr<2>(kv)), pkv(pr<4>(kv)), pkv(pr<6>(kv))};
    v4u o1 = {pkv(pr<8>(kv)), pkv(pr<10>(kv)), pkv(pr<12>(kv)), pkv(pr<14>(kv))};
    const u32 so = WS_KVP + (hd * 16 + tt) * 4096 + w * 2048;
    st128c(rWS, lane * 16, so, o0); st128c(rWS, lane * 16 + 1024, so, o1);
  }
  STAMP(7);
  RELANE();
  const u32 sl_w = lds0 + w * 1024;
  f32x16 accQ = {};
  v4u wmN[8];
  {
    const u32 r8 = (tid >> 2) * 8;
    const u32 voN = p0n ? r8 * 5376 + chunkN * 64 + (tid & 3) * 16 : 0x7ff00000;
    const u32 rr = lane >> 3;
    const u32 vO = rr * 896 + cs * 128 + (((lane & 7) ^ (((rr >> 1) & 1) << 2)) * 16), sgO = w * 7168;
    const u32 vR = rr * 896 + cs * 128 + (((lane & 7) ^ (rr >> 1)) * 16), sgR = (t64 * 64 + w * 16) * 896;
    const u32 bq = opq(L_WQ + (4 * h + q4) * 64 + g16 * 32 + ((p4 ^ (g16 & (p4 >> 1))) * 8));
    v4u fq[4];
#pragma unroll
    for (int s = 0; s < 3; ++s) fq[s] = L.tr2(bq + s * 1024, bq + s * 1024 + 512);
    sfor<0, 28>([&](auto sc) {
      constexpr int s = sc.value;
      accQ = mfma(fq[s & 3], H[s], accQ);
      SCHED();
      if constexpr (s + 3 < 28) fq[(s + 3) & 3] = L.tr2(bq + (s + 3) * 1024, bq + (s + 3) * 1024 + 512);
      if constexpr (s < 14) dma<1, L_WO + s * 4096, 0, s * 28672, 0>(vO, rWO, sgO, sl_w);
      if constexpr (s == 14) dma<1, L_XR, 0, 0, 0>(vR, rX, sgR, lds0 + w * 2048);
      if constexpr (s == 15) dma<1, L_XR + 1024, 0, 8 * 896, 0>(vR ^ 64, rX, sgR, lds0 + w * 2048);
      if constexpr (s >= 16 && s < 24) ald128<0>(wmN[s - 16], voN, rWM, (s - 16) * 5376);
      SCHED();
    });
  }
  STAMP(23);
  if (w < 2) VMW(24);
  BAR();
  if (tid == 0) set_flag(rWS, FL_KV + hd * 16 + tt, tlo, thi);
  STAMP(8);
  RELANE();
  const u32 rrD = lane >> 3;
  const u32 vW3 = rrD * 3584 + fs3 * 256 + ((((lane & 7) >> 1) ^ ((rrD >> 1) & 1) ^ (w & 2)) * 32) + (lane & 1) * 16;
  const u32 vD = rrD * 896 + cs * 128 + (((lane & 7) ^ (((rrD >> 1) & 1) << 2)) * 16);
  const u32 sg3 = (w & 1) * 32 * 3584, sgD = ((w >> 1) * 896 + (w & 1) * 32) * 896, slR = lds0 + w * 4096;
  const rsrc_t rW3 = w < 2 ? rWG : rWU;
  auto ring = [&](auto nc_, auto jc_, auto cc_) {
    constexpr int n = nc_.value, j = jc_.value, c = cc_.value;
    if constexpr (n < 14) dma<c, (n % 9) * SLOT + j * 1024, 1024, (64 * (n % 7) + 8 * j) * 3584 + (n / 7) * 128, 8 * 3584>(vW3, rW3, sg3, slR);
    else dma<c, (n % 9) * SLOT + j * 1024, 1024, (((n - 14) % 7) * 128 + ((n - 14) / 7) * 64 + 8 * j) * 896, 8 * 896>(vD, rWD, sgD, slR);
  };
  u32 Q1[8], Q2[8];
  {
    ring(IC<0>{}, IC<0>{}, IC<4>{});
    v4u b0, b1;
    epi_t(accQ, b0, b1);
    f32x16 pq = {};
    pq = mfma(PH[0], b0, pq); pq = mfma(PH[1], b1, pq);
    SCHED();
    ring(IC<1>{}, IC<0>{}, IC<4>{});
    feature_map(pq, h, Q1, Q2);
  }
  asm volatile("s_waitcnt vmcnt(8)" : "+v"(wmN[0]), "+v"(wmN[1]), "+v"(wmN[2]), "+v"(wmN[3]), "+v"(wmN[4]), "+v"(wmN[5]), "+v"(wmN[6]), "+v"(wmN[7])::"memory");
  if (p0n) p0_finish(L.r128(L_SIL + (tid >> 2) * 16), wmN, chunkN);
  STAMP(9);
  if (w == 0) wait_flags(rWS, WS_FLAG + (FL_KV + hd * 16 + (lane & 15)) * 256, lane < 16, tlo, thi, 3);
  BAR();
  STAMP(10);
  RELANE();
  {
    v4u pp[16];
    const u32 so = WS_KVP + hd * 16 * 4096 + w * 1024;
#pragma unroll
    for (int i = 0; i < 16; ++i) pp[i] = ld128c(rWS, lane * 16, so + i * 4096);
    float sv[8] = {};
    u32 selLo, selHi;
    asm("s_mov_b32 %0, 0x3f80" : "=s"(selLo));
    asm("s_mov_b32 %0, 0x3f800000" : "=s"(selHi));
#pragma unroll
    for (int i = 0; i < 16; ++i)
#pragma unroll
      for (int e = 0; e < 4; ++e) { sv[2 * e] = dot2(pp[i][e], selLo, sv[2 * e]); sv[2 * e + 1] = dot2(pp[i][e], selHi, sv[2 * e + 1]); }
    v4u o = {pk2(sv[0], sv[1]), pk2(sv[2], sv[3]), pk2(sv[4], sv[5]), pk2(sv[6], sv[7])};
    L.w128(L_KVR + w * 1024 + lane * 16, o);
  }
  BAR();
  STAMP(11);
  {
    f32x16 o = {};
    const u32 kb = opq(L_KVR + lane * 16);
    v4u b0 = {Q1[0], Q1[1], Q1[2], Q1[3]}, b1 = {Q1[4], Q1[5], Q1[6], Q1[7]}, b2 = {Q2[0], Q2[1], Q2[2], Q2[3]}, b3 = {Q2[4], Q2[5], Q2[6], Q2[7]};
    o = mfma(L.r128(kb), b0, o);
    o = mfma(L.r128(kb + 1024), b1, o);
    o = mfma(L.r128(kb + 2048), b2, o);
    o = mfma(L.r128(kb + 3072), b3, o);
    const float den = half_hi(o[12]) + 1e-6f;
    const float rd = __builtin_amdgcn_rcpf(den), rd2 = h ? 0.f : rd;
    const f32x2 r1 = bc(rd), r2 = bc(rd2);
    v4u o0 = {pkv(pr<0>(o) * r1), pkv(pr<2>(o) * r1), pkv(pr<4>(o) * r1), pkv(pr<6>(o) * r1)};
    v4u o1 = {pkv(pr<8>(o) * r1), pkv(pr<10>(o) * r1), pkv(pr<12>(o) * r2), pkv(pr<14>(o) * r2)};
    const u32 so = WS_OFR + ((tt * 4 + w) * 32 + hd * 2) * 1024;
    st128c(rWS, lane * 16, so, o0); st128c(rWS, lane * 16 + 1024, so, o1);
  }
  const u32 vtg = ((w == 0 ? 896 : (w == 1 ? 1792 : 2240)) + cs * 64 + lane) * 8;
  v2u mtg;
  ald64c(mtg, vtg, rWS, so_mod);
  ring(IC<2>{}, IC<0>{}, IC<4>{}); ring(IC<3>{}, IC<0>{}, IC<4>{});
  asm volatile("s_waitcnt vmcnt(8)" : "+v"(mtg)::"memory");
  BAR();
  if (tid == 0) set_flag(rWS, FL_O + tt * 16 + hd, tlo, thi);
  if (w < 3) {
    for (int spins = 0;; ++spins) {
      if (__all((mtg.x & 0xffff0000u) == glo && mtg.y == ghi)) break;
      if (spins > SPIN_CAP) { st32c(rWS, WS_TMO, 0, 2); break; }
      asm volatile("" ::: "memory");
      mtg = ld64c(rWS, vtg, so_mod);
    }
    L.wf(L_TGA + tid * 4, bflo(mtg.x));
  }
  STAMP(12);
  RELANE();
  if (!p24) {
    const int rank = (iq - 28) * 4 + (xq >> 1);
    if (rank < 14) {
      const int mat = rank / 7, f3 = 7 * (xq & 1) + rank % 7;
      VMW(0);
      BAR();
      {
        const u32 rr = lane >> 3;
        const u32 v = rr * 3584 + f3 * 256 + (((lane & 7) ^ (((rr >> 1) & 1) << 2)) * 16);
        dma<28, L_CI, 2048, 0, 16 * 3584>(v, mat ? rWU : rWG, (w >> 1) * 128 + (w & 1) * 8 * 3584, lds0 + (w >> 1) * 57344 + (w & 1) * 1024);
      }
      {
        const bool act = tid < 224;
        v4u a, b;
        for (int spins = 0;; ++spins) {
          asm volatile("" ::: "memory");
          a = ld128c(rWS, tid * 16 + 1792 * 8, so_mod); b = ld128c(rWS, tid * 16 + 1344 * 8, so_mod);
          const bool ok = !act || (gran_ok(a, glo, ghi) && gran_ok(b, glo, ghi));
          if (__all(ok)) break;
          if (spins > SPIN_CAP) { st32c(rWS, WS_TMO, 0, 7); break; }
        }
        const u32 kk = (2 * tid) & 15;
        const u32 o = L_CT + (tid >> 3) * 64 + ((kk >> 2) & 1) * 16 + ((kk & 3) + 4 * (kk >> 3)) * 2;
        if (act) {
          *(LAS u32*)(L.p + o) = (a.x & 0xffffu) | (a.z << 16);
          *(LAS u32*)(L.p + o + 32) = (b.x & 0xffffu) | (b.z << 16);
        }
      }
      VMW(0);
      BAR();
      f32x16 acc = {};
      const u32 sw = ((q4 >> 1) & 1) << 2;
      const u32 cb = opq(L_CI + (w >> 1) * 57344 + (4 * h + q4) * 128 + (((4 * (w & 1) + 2 * g16 + (p4 >> 1)) ^ sw) * 16) + (p4 & 1) * 8);
      const u32 tb = opq(L_CT + (lane & 1) * 32 + h * 16);
#pragma unroll
      for (int s = 0; s < 28; ++s) acc = mfma(L.tr2(cb + s * 2048, cb + s * 2048 + 1024), L.r128(tb + s * 64), acc);
      if (t32 < 2) {
#pragma unroll
        for (int qq = 0; qq < 4; ++qq) {
          f32x4 v = {acc[4 * qq], acc[4 * qq + 1], acc[4 * qq + 2], acc[4 * qq + 3]};
          L.wf4(L_CB + t32 * 512 + (w * 32 + 8 * qq + 4 * h) * 4, v);
        }
      }
      BAR();
      if (tid < 128) {
        u32 ch, cl, bh, bl;
        split2(L.rf(L_CB + tid * 4), ch, cl); split2(L.rf(L_CB + 512 + tid * 4), bh, bl);
        v4u e = {ch | (cl << 16), ch | (bh << 16), bl | (bh << 16), 0u};
        st128c(rWS, (tid >> 4) * 512 + (tid & 15) * 16, WS_EXT + f3 * 4096 + mat * 256, e);
      }
      VMW(0);
      BAR();
      if (tid == 0) set_flag(rWS, FL_C + mat * 14 + f3, tlo, thi);
    }
    return;
  }

  u32 x1p[8];
  const int tt2 = w & 1, kh = w >> 1;
  const u32 T32 = t64 * 2 + tt2;
  {
    if (w == 0) wait_flags(rWS, WS_FLAG + (FL_O + (t64 >> 1) * 16 + (lane & 15)) * 256, lane < 16, tlo, thi, 4);
    BAR();
    if (bid == 0 && tid == 0) { v2u t = {tlo, thi}; st64c(rWS, WS_EPOCH, 0, t); }
    STAMP(13);
    v4u O[16];
    {
      const u32 so = WS_OFR + (T32 * 32 + kh * 16) * 1024;
#pragma unroll
      for (int i = 0; i < 16; ++i) O[i] = ld128c(rWS, lane * 16 + (i & 3) * 1024, so + (i >> 2) * 4096);
    }
    f32x16 acc[2] = {};
    {
      const u32 sw = ((q4 >> 1) & 1) << 2;
      const u32 cb0 = (((2 * g16 + (p4 >> 1)) ^ sw) * 16) + (p4 & 1) * 8, cb1 = (((4 + 2 * g16 + (p4 >> 1)) ^ sw) * 16) + (p4 & 1) * 8;
      const u32 rb = L_WO + kh * 8 * 28 * 128 + q4 * 128;
      const u32 bh0 = opq(rb + 4 * h * 128 + cb0), bh1 = opq(rb + 4 * h * 128 + cb1), bn0 = opq(rb + cb0), bn1 = opq(rb + cb1);
      auto rd = [&](int i, int ct) -> v4u {
        const int hh = i >> 1, s = i & 1;
        const u32 o0 = (28 * hh + 16 * s) * 128, o1 = o0 + 8 * 128;
        const u32 b = ct ? bh1 : bh0, bn = ct ? bn1 : bn0;
        return L.tr2(b + o0, (s ? bn : b) + o1);
      };
      v4u f[3][2];
      f[0][0] = rd(0, 0); f[0][1] = rd(0, 1); f[1][0] = rd(1, 0); f[1][1] = rd(1, 1);
#pragma unroll
      for (int i = 0; i < 16; ++i) {
        acc[0] = mfma(f[i % 3][0], O[i], acc[0]);
        SCHED();
        if (i + 2 < 16) f[(i + 2) % 3][0] = rd(i + 2, 0);
        SCHED();
        acc[1] = mfma(f[i % 3][1], O[i], acc[1]);
        SCHED();
        if (i + 2 < 16) f[(i + 2) % 3][1] = rd(i + 2, 1);
        SCHED();
      }
    }
    STAMP(14);
    {
      const f32x16 snd = kh ? acc[0] : acc[1];
      const u32 xb = opq(L_XCH + w * 4096 + lane * 16);
#pragma unroll
      for (int qq = 0; qq < 4; ++qq) { f32x4 v = {snd[4 * qq], snd[4 * qq + 1], snd[4 * qq + 2], snd[4 * qq + 3]}; L.wf4(xb + qq * 1024, v); }
    }
    BAR();
    {
      const f32x16 mine = kh ? acc[1] : acc[0];
      const u32 xb = opq(L_XCH + (w ^ 2) * 4096 + lane * 16);
      const u32 row = tt2 * 32 + t32, rsw = (row >> 1) & 7;
      const u32 xrb = opq(L_XR + row * 128 + 8 * h), tg = opq(L_TGA + (kh * 32 + 4 * h) * 4);
      f32x2 x1[8];
      u32 a2[8];
      f32x2 sum = {0.f, 0.f};
      sfor<0, 4>([&](auto qq) {
        constexpr int Q = qq.value;
        const f32x4 o = L.rf4(xb + Q * 1024);
        const v2u xr = L.r64(xrb + (((kh * 4 + Q) ^ rsw) * 16));
        const f32x4 ga4 = L.rf4(tg + 32 * Q), g24 = L.rf4(tg + 256 + 32 * Q);
        x1p[2 * Q] = pkv(fma2(lo2(ga4), pr<4 * Q>(mine) + lo2(o), unp(xr.x)));
        x1p[2 * Q + 1] = pkv(fma2(hi2(ga4), pr<4 * Q + 2>(mine) + hi2(o), unp(xr.y)));
        x1[2 * Q] = unp(x1p[2 * Q]); x1[2 * Q + 1] = unp(x1p[2 * Q + 1]);
        a2[2 * Q] = pkv(x1[2 * Q] * lo2(g24)); a2[2 * Q + 1] = pkv(x1[2 * Q + 1] * hi2(g24));
        sum += x1[2 * Q] + x1[2 * Q + 1];
      });
      v4u o0 = {a2[0], a2[1], a2[2], a2[3]}, o1 = {a2[4], a2[5], a2[6], a2[7]};
      const u32 so = WS_X1F + (T32 * 28 + cs * 4 + kh * 2) * 1024;
      st128c(rWS, lane * 16, so, o0); st128c(rWS, lane * 16 + 1024, so, o1);
      const float mean = half_sum(sum.x + sum.y) * (1.f / 32.f);
      f32x2 m2v = {0.f, 0.f};
#pragma unroll
      for (int r = 0; r < 8; ++r) { const f32x2 d = x1[r] - bc(mean); m2v = fma2(d, d, m2v); }
      const float m2 = half_sum(m2v.x + m2v.y);
      if (h == 0) { v2u st = {__float_as_uint(mean), __float_as_uint(m2)}; st64c(rWS, t32 * 8, WS_STAT + (T32 * 14 + cs * 2 + kh) * 256, st); }
    }
  }
  ring(IC<4>{}, IC<0>{}, IC<4>{}); ring(IC<5>{}, IC<0>{}, IC<4>{}); ring(IC<6>{}, IC<0>{}, IC<4>{});
  VMW(12);
  BAR();
  if (tid == 0) set_flag(rWS, FL_X + t64 * 7 + cs, tlo, thi);
  STAMP(15);
  ring(IC<7>{}, IC<0>{}, IC<4>{}); ring(IC<8>{}, IC<0>{}, IC<4>{});
  if (w == 0) {
    const u32 fo = lane < 14 ? FL_X + (tt3 * 2 + lane / 7) * 7 + lane % 7 : FL_C + (lane - 14) * 14 + fs3;
    wait_flags(rWS, WS_FLAG + fo * 256, lane < 16, tlo, thi, 5);
  }
  BAR();
  STAMP(16);
  RELANE();
  v4u B[28];
  const u32 T32p = tt3 * 4 + w;
  v2u st[7];
  const u32 sx = WS_X1F + T32p * 28 * 1024, vx = lane * 16;
  dma<1, L_EXT, 0, WS_EXT, 0>(lane * 16, rWS, fs3 * 4096 + w * 1024, lds0 + w * 1024);
  {
    const u32 vs = t32 * 8 + h * 7 * 256, so = WS_STAT + T32p * 14 * 256;
    sfor<0, 7>([&](auto i) { ald64c(st[i.value], vs, rWS, so + i.value * 256); });
    sfor<0, NB0>([&](auto s) { ald128c<(s.value & 3) * 1024>(B[s.value], vx, rWS, sx + (s.value >> 2) * 4096); });
  }
  float rs2;
  v4u Bx;
  auto ln2_stats = [&]() {
    asm volatile("s_waitcnt vmcnt(%7)" : "+v"(st[0]), "+v"(st[1]), "+v"(st[2]), "+v"(st[3]), "+v"(st[4]), "+v"(st[5]), "+v"(st[6]) : "n"(PL.wS) : "memory");
    float mean = 0.f, m2 = 0.f;
#pragma unroll
    for (int i = 0; i < 7; ++i) mean += __uint_as_float(st[i].x);
    mean = half_sum(mean) * (1.f / 14.f);
#pragma unroll
    for (int i = 0; i < 7; ++i) { const float d = __uint_as_float(st[i].x) - mean; m2 += fmaf(32.f * d, d, __uint_as_float(st[i].y)); }
    const float var = half_sum(m2) * (1.f / 448.f) + 1e-5f;
    const float rstd2 = __builtin_amdgcn_rsqf(var);
    rs2 = rstd2 * rstd2;
    u32 mh, ml, sh, sl_;
    split2(-mean, mh, ml); split2(var * rstd2, sh, sl_);
    Bx = h ? v4u{0u, 0u, 0u, 0u} : v4u{mh | (mh << 16), ml | (sh << 16), sh | (sl_ << 16), 0u};
  };
  STAMP(17);
  v4u A_[56];
  const u32 sa0 = WS_ACT + (T32 * 112 + kh * 56) * 1024;
  auto a_load = [&](auto jc) {
    constexpr int j = jc.value;
    ald128c<(j & 3) * 1024>(A_[j], vx, rWS, sa0 + ((j % 28) >> 2) * 8192 + (j / 28) * 4096);
  };
  const u32 vfl = WS_FLAG + (FL_A + ((t64 >> 1) * 14 + (lane < 14 ? lane : 0)) * 2) * 256;
  v2u fa;
  {
    const u32 so_act = WS_ACT + (T32p * 112 + fs3 * 8) * 1024;
    u32 bA[4], bo[4];
#pragma unroll
    for (int i = 0; i < 4; ++i) bo[i] = g16 * 8192 + (4 * h + q4) * 128 + ((i ^ ((q4 >> 1) & 1) ^ (g16 * 2)) * 32) + p4 * 8;
    const u32 eb = opq(L_EXT + t32 * 16);
    int curgrp = -1;
    (void)curgrp;
    v4u f[2][4];
    auto setbase = [&](auto gc_) {
#pragma unroll
      for (int i = 0; i < 4; ++i) bA[i] = opq(bo[i] + gc_.value * 65536);
    };
    auto rdf = [&](auto Gc, auto sc, int i) -> v4u {
      constexpr int G = Gc.value, s = sc.value;
      if constexpr (s == 28) return L.r128(eb + (G * 4 + i) * 512);
      else {
        constexpr int slot = (G * 7 + s / 4) % 9;
        const u32 b = bA[i] + (slot & 3) * SLOT + (s & 3) * 2048;
        return L.tr2(b, b + 1024);
      }
    };
    setbase(IC<0>{});
#pragma unroll
    for (int i = 0; i < 4; ++i) f[0][i] = rdf(IC<0>{}, IC<0>{}, i);
    sfor<0, 2>([&](auto Gc) {
      constexpr int G = Gc.value;
      f32x16 acc[4] = {};
      sfor<0, 29>([&](auto sc) {
        constexpr int s = sc.value, par = (G * 29 + s) & 1;
        constexpr int pos = G * 7 + (s < 28 ? s / 4 : 6);
        constexpr int nG = s + 1 < 29 ? G : G + 1, ns = s + 1 < 29 ? s + 1 : 0;
        constexpr bool nx = nG < 2;
        if constexpr (nx && ns < 28) {
          constexpr int sl_n = (nG * 7 + ns / 4) % 9, sl_p = (ns == 0) ? (G * 7 + 6) % 9 : (nG * 7 + (ns - 1) / 4) % 9;
          if constexpr ((sl_n >> 2) != (sl_p >> 2)) setbase(IC<(sl_n >> 2)>{});
        }
        if constexpr (G == 0 && s < 28) asm volatile("s_waitcnt vmcnt(%1)" : "+v"(B[s]) : "n"(PL.wB[s]) : "memory");
        if constexpr (G == 1 && s == PA_I) { if (w == 0) ald64c(fa, vfl, rWS, zero); }
#pragma unroll
        for (int i = 0; i < 4; ++i) {
          if constexpr (s < 28) acc[i] = mfma(f[par][i], B[s], acc[i]); else acc[i] = mfma(f[par][i], Bx, acc[i]);
          if constexpr (nx) f[par ^ 1][i] = rdf(IC<nG>{}, IC<ns>{}, i);
          if constexpr (s < 28 && pos >= 1) { if (i == 0) ring(IC<pos + 8>{}, IC<(s & 3)>{}, IC<1>{}); }
          if constexpr (G == 0 && s + NB0 < 28) { if (i == 2) ald128c<((s + NB0) & 3) * 1024>(B[s + NB0], vx, rWS, sx + ((s + NB0) >> 2) * 4096); }
          if constexpr (G == 1 && s > PA_C && s < 28) { if (i == 2) a_load(IC<s - PA_C - 1>{}); }
          if constexpr (G == 0 && s == SST) { if (i == 3) ln2_stats(); }
          SCHED();
        }
        if constexpr (s < 28 && (s & 3) == 3) {
          if constexpr (G == 1 && s == PA_C) {
            if (w == 0) {
              asm volatile("s_waitcnt vmcnt(%1)" : "+v"(fa) : "n"(PL.wPollA) : "memory");
              if (!__all(fa.x == tlo && fa.y == thi)) wait_flags(rWS, vfl, true, tlo, thi, 6);
            }
          }
          if constexpr (PL.wBar[pos] < 63) VMW(PL.wBar[pos]);
          SBAR();
          if constexpr (pos == 8) { if (tid == 0) set_flag(rWS, FL_A + (tt3 * 14 + fs3) * 2, tlo, thi); }
        }
      });
      const f32x2 r2 = bc(rs2);
#pragma unroll
      for (int i = 0; i < 4; ++i) {
        const f32x16& a = acc[i];
        v4u ov = {pkv(pr<0>(a) * pr<8>(a) * r2), pkv(pr<2>(a) * pr<10>(a) * r2), pkv(pr<4>(a) * pr<12>(a) * r2), pkv(pr<6>(a) * pr<14>(a) * r2)};
        st128c(rWS, lane * 16 + i * 1024, so_act + G * 4096, ov);
      }
      STAMP(18 + G);
    });
  }
  {
    STAMP(20);
    f32x16 acc[2] = {};
    const u32 sw = ((q4 >> 1) & 1) << 2;
    const u32 rb = kh * 8192 + (4 * h + q4) * 128 + (p4 & 1) * 8;
    const u32 co0 = rb + (((2 * g16 + (p4 >> 1)) ^ sw) * 16), co1 = rb + (((4 + 2 * g16 + (p4 >> 1)) ^ sw) * 16);
    u32 c0, c1;
    auto setb = [&](auto gc_) { c0 = opq(co0 + gc_.value * 65536); c1 = opq(co1 + gc_.value * 65536); };
    auto rd = [&](auto oc, int ct) -> v4u {
      constexpr int o = oc.value, slot = (14 + o / 4) % 9;
      const u32 b = (ct ? c1 : c0) + (slot & 3) * SLOT + (o & 3) * 2048;
      return L.tr2(b, b + 1024);
    };
    setb(IC<((14 % 9) >> 2)>{});
    v4u f[3][2];
    f[0][0] = rd(IC<0>{}, 0); f[0][1] = rd(IC<0>{}, 1); f[1][0] = rd(IC<1>{}, 0); f[1][1] = rd(IC<1>{}, 1);
    v2u fb;
    sfor<0, 56>([&](auto oc) {
      constexpr int o = oc.value, pos = 14 + o / 4;
      if constexpr (o + 2 < 56) {
        constexpr int sl_n = (14 + (o + 2) / 4) % 9, sl_p = (14 + (o + 1) / 4) % 9;
        if constexpr ((sl_n >> 2) != (sl_p >> 2)) setb(IC<(sl_n >> 2)>{});
      }
      if constexpr (o == 4) STAMP(24);
      if constexpr (o == 16) STAMP(25);
      if constexpr (o == 28) STAMP(26);
      if constexpr (o == 44) STAMP(27);
      if constexpr (o == PB_I) { if (w == 0) ald64c(fb, vfl + 256, rWS, zero); }
      asm volatile("s_waitcnt vmcnt(%1)" : "+v"(A_[o]) : "n"(PL.wAct[o]) : "memory");
      acc[0] = mfma(f[o % 3][0], A_[o], acc[0]);
      SCHED();
      if constexpr (o + 2 < 56) f[(o + 2) % 3][0] = rd(IC<o + 2>{}, 0);
      if constexpr (o < 24) ring(IC<pos + 8>{}, IC<(o & 3)>{}, IC<1>{});
      SCHED();
      acc[1] = mfma(f[o % 3][1], A_[o], acc[1]);
      SCHED();
      if constexpr (o + 2 < 56) f[(o + 2) % 3][1] = rd(IC<o + 2>{}, 1);
      if constexpr (o + AW < 56) a_load(IC<o + AW>{});
      SCHED();
      if constexpr ((o & 3) == 3 && pos <= 25) {
        if constexpr (o == PB_C) {
          if (w == 0) {
            asm volatile("s_waitcnt vmcnt(%1)" : "+v"(fb) : "n"(PL.wPollB) : "memory");
            if (!__all(fb.x == tlo && fb.y == thi)) wait_flags(rWS, vfl + 256, true, tlo, thi, 8);
          }
        }
        if constexpr (PL.wBar[pos] < 63) VMW(PL.wBar[pos]);
        SBAR();
        if constexpr (pos == 14) { if (tid == 0) set_flag(rWS, FL_A + (tt3 * 14 + fs3) * 2 + 1, tlo, thi); }
      }
    });
    STAMP(21);
    BAR();
    {
      const f32x16 snd = kh ? acc[0] : acc[1];
      const u32 xb = L_XC4 + w * 4096 + lane * 16;
#pragma unroll
      for (int qq = 0; qq < 4; ++qq) { f32x4 v = {snd[4 * qq], snd[4 * qq + 1], snd[4 * qq + 2], snd[4 * qq + 3]}; L.wf4(xb + qq * 1024, v); }
    }
    BAR();
    RELANE();
    {
      const f32x16 mine = kh ? acc[1] : acc[0];
      const u32 xb = L_XC4 + (w ^ 2) * 4096 + lane * 16;
      const u32 row = tt2 * 32 + t32, rsw = (row >> 1) & 7;
      const u32 ob = L_OUT + row * 128 + 8 * h, tg = opq(L_TGB + (kh * 32 + 4 * h) * 4);
      sfor<0, 4>([&](auto qq) {
        constexpr int Q = qq.value;
        const f32x4 o = L.rf4(xb + Q * 1024);
        const f32x4 gb4 = L.rf4(tg + 32 * Q);
        v2u r = {pkv(fma2(lo2(gb4), pr<4 * Q>(mine) + lo2(o), unp(x1p[2 * Q]))), pkv(fma2(hi2(gb4), pr<4 * Q + 2>(mine) + hi2(o), unp(x1p[2 * Q + 1])))};
        L.w64(ob + (((kh * 4 + Q) ^ rsw) * 16), r);
      });
    }
    BAR();
    {
      const u32 rr = lane >> 3, c = lane & 7;
#pragma unroll
      for (int j = 0; j < 2; ++j) {
        const u32 row = w * 16 + 8 * j + rr;
        const v4u v = L.r128(L_OUT + row * 128 + c * 16);
        __builtin_amdgcn_raw_buffer_store_b128(v, rOUT, row * 896 + ((c ^ ((row >> 1) & 7)) * 16), (t64 * 64) * 896 + cs * 128, 0);
      }
    }
    STAMP(22);
    asm volatile(".Lfb_code1:");
  }
}

extern "C" void kernel_launch(void* const* d_in, const int* in_sizes, int n_in, void* d_out, int out_size, void* d_ws, size_t ws_size,
                              hipStream_t stream) {
  (void)in_sizes; (void)n_in; (void)out_size; (void)ws_size;
  (void)hipFuncSetAttribute((const void*)fused_block, hipFuncAttributeMaxDynamicSharedMemorySize, LDS_BYTES);
  fused_block<<<256, 256, LDS_BYTES, stream>>>((const u16*)d_in[0], (const u16*)d_in[1], (const u16*)d_in[2], (const u16*)d_in[3],
                                               (const u16*)d_in[5], (const u16*)d_in[6], (const u16*)d_in[7], (const u16*)d_in[8],
                                               (const u16*)d_in[9], (u16*)d_out, (unsigned char*)d_ws);
}
```

```cpp
#include <hip/hip_runtime.h>
#include <stdint.h>

typedef unsigned u32;
typedef unsigned long long u64;
typedef unsigned short u16;
typedef u32 v4u __attribute__((ext_vector_type(4)));
typedef u32 v2u __attribute__((ext_vector_type(2)));
typedef float f32x2 __attribute__((ext_vector_type(2)));
typedef float f32x4 __attribute__((ext_vector_type(4)));
typedef float f32x16 __attribute__((ext_vector_type(16)));
typedef __bf16 bf16x2 __attribute__((ext_vector_type(2)));
typedef __bf16 bf16x8 __attribute__((ext_vector_type(8)));
typedef short s16x4 __attribute__((ext_vector_type(4)));
typedef __amdgpu_buffer_rsrc_t rsrc_t;
#define LAS __attribute__((address_space(3)))
#define DI __device__ __forceinline__
#define OUT_AUX 16

constexpr u32 WS_EPOCH = 0;
constexpr u32 WS_TMO = 256;
constexpr u32 WS_FLAG = 4096;
constexpr u32 FL_KV = 0, FL_O = 512, FL_X = 768, FL_C = 1024, FL_A = 1280;
constexpr u32 WS_MODG = 0x80000;
constexpr u32 MODG_REP = 2688 * 8;
constexpr u32 WS_EXT = 0xB0000;
constexpr u32 WS_STAT = 0xC0000;
constexpr u32 WS_KVP = 0x100000;
constexpr u32 WS_OFR = 0x200000;
constexpr u32 WS_X1F = 0x400000;
constexpr u32 WS_ACT = 0x600000;
constexpr u32 WS_USED = 0x1000000;

constexpr u32 SLOT = 16384;
constexpr u32 L_XS = 0;
constexpr u32 L_WKV1 = 114688;
constexpr u32 L_WKV2 = 65536;
constexpr u32 L_WQ = 32768;
constexpr u32 L_PHI = 0, L_VFR = 16384;
constexpr u32 L_WO = 65536, L_XCH = 122880, L_XR = 139264;
constexpr u32 L_T1 = 147456;
constexpr u32 L_KVR = 147456;
constexpr u32 L_WPH = 154624;
constexpr u32 L_P0S = 156672;
constexpr u32 L_EXT = 154624;
constexpr u32 L_ST1 = 158720;
constexpr u32 L_TGA = 159232, L_TG2 = 159488, L_TGB = 159744;
constexpr u32 L_CI = 0, L_CT = 147456, L_CB = 149504;
constexpr u32 L_XC4 = 5 * SLOT, L_OUT = 6 * SLOT;
constexpr u32 L_SIL = 160768;
constexpr u32 L_DUMP = 122880;
constexpr u32 LDS_BYTES = 163840;

constexpr int SPIN_CAP = 40000;
#define LEXP(c) __builtin_expect((c), 1)

template <int V> struct IC { static constexpr int value = V; };
template <int I, int N, class F> DI void sfor(F&& f) { if constexpr (I < N) { f(IC<I>{}); sfor<I + 1, N>(f); } }

DI rsrc_t mk(const void* p, u32 bytes) { return __builtin_amdgcn_make_buffer_rsrc((void*)p, 0, (int)bytes, 0x00020000); }
DI float bflo(u32 v) { return __uint_as_float(v << 16); }
DI float bfhi(u32 v) { return __uint_as_float(v & 0xffff0000u); }
DI f32x2 unp(u32 v) { f32x2 r = {bflo(v), bfhi(v)}; return r; }
DI u32 pkv(f32x2 v) { return __builtin_bit_cast(u32, __builtin_convertvector(v, bf16x2)); }
DI u32 pk2(float a, float b) { f32x2 v = {a, b}; return pkv(v); }
DI float rbf(float a) { return bflo(pk2(a, a)); }
DI f32x2 bc(float a) { f32x2 r = {a, a}; return r; }
DI f32x2 fma2(f32x2 a, f32x2 b, f32x2 c) { return __builtin_elementwise_fma(a, b, c); }
DI f32x2 lo2(f32x4 v) { f32x2 r = {v[0], v[1]}; return r; }
DI f32x2 hi2(f32x4 v) { f32x2 r = {v[2], v[3]}; return r; }
template <int R> DI f32x2 pr(const f32x16& a) { f32x2 r = {a[R], a[R + 1]}; return r; }
DI float dot2(u32 a, u32 b, float c) { return __builtin_amdgcn_fdot2_f32_bf16(__builtin_bit_cast(bf16x2, a), __builtin_bit_cast(bf16x2, b), c, false); }
DI bf16x8 asb(v4u v) { return __builtin_bit_cast(bf16x8, v); }
DI f32x16 mfma(v4u a, v4u b, f32x16 c) { return __builtin_amdgcn_mfma_f32_32x32x16_bf16(asb(a), asb(b), c, 0, 0, 0); }
DI float half_sum(float v) { auto s = __builtin_amdgcn_permlane32_swap(__float_as_uint(v), __float_as_uint(v), false, false); return __uint_as_float(s[0]) + __uint_as_float(s[1]); }
DI float half_max(float v) { auto s = __builtin_amdgcn_permlane32_swap(__float_as_uint(v), __float_as_uint(v), false, false); return fmaxf(__uint_as_float(s[0]), __uint_as_float(s[1])); }
DI float half_min(float v) { auto s = __builtin_amdgcn_permlane32_swap(__float_as_uint(v), __float_as_uint(v), false, false); return fminf(__uint_as_float(s[0]), __uint_as_float(s[1])); }
DI float half_hi(float v) { auto s = __builtin_amdgcn_permlane32_swap(__float_as_uint(v), __float_as_uint(v), false, false); return __uint_as_float(s[1]); }
template <int CTRL> DI float dpp_add(float v) { return v + __int_as_float(__builtin_amdgcn_update_dpp(0, __float_as_int(v), CTRL, 0xf, 0xf, true)); }
#define ROR2 0x122
#define ROR4 0x124
#define ROR8 0x128
#define BAR() asm volatile("s_waitcnt lgkmcnt(0)\n\ts_barrier" ::: "memory")
#define VMW(n) asm volatile("s_waitcnt vmcnt(%0)" ::"n"(n) : "memory")
#define SCHED() __builtin_amdgcn_sched_barrier(0)

template <int N, int L0, int LS, int G0, int GS> DI void dma(u32 voff, rsrc_t rs, u32 sg, u32 sl) {
  u32 t;
  asm volatile("s_add_u32 m0, %3, %5\n\ts_add_u32 %0, %2, %6\n\tbuffer_load_dwordx4 %1, %4, %0 offen lds\n\t"
               ".rept %7\n\ts_add_u32 m0, m0, %8\n\ts_add_u32 %0, %0, %9\n\tbuffer_load_dwordx4 %1, %4, %0 offen lds\n\t.endr"
               : "=&s"(t) : "v"(voff), "s"(sg), "s"(sl), "s"(rs), "n"(L0), "n"(G0), "n"(N - 1), "n"(LS), "n"(GS) : "memory", "scc");
}
template <int R> DI void dma_x4(u32 voff, rsrc_t rs, u32 sg, u32 sl) {
  u32 t;
  asm volatile("s_add_u32 m0, %3, %5\n\ts_add_u32 %0, %2, %5\n\tbuffer_load_dwordx4 %1, %4, %0 offen lds\n\t"
               "s_add_u32 m0, m0, 896\n\ts_add_u32 %0, %0, 896\n\tbuffer_load_dwordx4 %1, %4, %0 offen lds\n\t"
               "s_add_u32 m0, m0, 13440\n\ts_add_u32 %0, %0, 13440\n\tbuffer_load_dwordx4 %1, %4, %0 offen lds\n\t"
               "s_add_u32 m0, m0, 896\n\ts_add_u32 %0, %0, 896\n\tbuffer_load_dwordx4 %1, %4, %0 offen lds"
               : "=&s"(t) : "v"(voff), "s"(sg), "s"(sl), "s"(rs), "n"(R * 896) : "memory", "scc");
}
DI void warm(u32 voff, rsrc_t rs, u32 sg, u32 sl) {
  asm volatile("s_add_u32 m0, %3, %4\n\ts_nop 0\n\tbuffer_load_dword %0, %1, %2 offen lds" ::"v"(voff), "s"(rs), "s"(sg), "s"(sl), "n"(L_DUMP) : "memory", "scc");
}
template <int L0, int G0> DI void dma3(u32 v0, u32 v1, u32 v2, rsrc_t rs, u32 sg, u32 sl) {
  u32 t;
  asm volatile("s_add_u32 m0, %5, %7\n\ts_add_u32 %0, %4, %8\n\tbuffer_load_dwordx4 %1, %6, %0 offen lds\n\t"
               "s_add_u32 m0, m0, 2048\n\ts_nop 0\n\tbuffer_load_dwordx4 %2, %6, %0 offen lds\n\t"
               "s_add_u32 m0, m0, 2048\n\ts_nop 0\n\tbuffer_load_dwordx4 %3, %6, %0 offen lds"
               : "=&s"(t) : "v"(v0), "v"(v1), "v"(v2), "s"(sg), "s"(sl), "s"(rs), "n"(L0), "n"(G0) : "memory", "scc");
}
template <int IMM> DI void ald128(v4u& d, u32 voff, rsrc_t rs, u32 soff) {
  asm volatile("buffer_load_dwordx4 %0, %1, %2, %3 offen offset:%4" : "=v"(d) : "v"(voff), "s"(rs), "s"(soff), "n"(IMM) : "memory");
}
template <int IMM> DI void ald128c(v4u& d, u32 voff, rsrc_t rs, u32 soff) {
  asm volatile("buffer_load_dwordx4 %0, %1, %2, %3 offen offset:%4 sc1" : "=v"(d) : "v"(voff), "s"(rs), "s"(soff), "n"(IMM) : "memory");
}
DI void ald32(u32& d, u32 voff, rsrc_t rs, u32 soff) { asm volatile("buffer_load_dword %0, %1, %2, %3 offen" : "=v"(d) : "v"(voff), "s"(rs), "s"(soff) : "memory"); }
DI void ald64(v2u& d, u32 voff, rsrc_t rs, u32 soff) { asm volatile("buffer_load_dwordx2 %0, %1, %2, %3 offen" : "=v"(d) : "v"(voff), "s"(rs), "s"(soff) : "memory"); }
DI void ald64c(v2u& d, u32 voff, rsrc_t rs, u32 soff) { asm volatile("buffer_load_dwordx2 %0, %1, %2, %3 offen sc1" : "=v"(d) : "v"(voff), "s"(rs), "s"(soff) : "memory"); }
#define TIE(x) asm volatile("" : "+v"(x))

DI v4u ld128(rsrc_t r, u32 off, u32 so = 0) { return __builtin_amdgcn_raw_buffer_load_b128(r, off, so, 0); }
DI v4u ld128c(rsrc_t r, u32 off, u32 so = 0) { return __builtin_amdgcn_raw_buffer_load_b128(r, off, so, 16); }
DI v2u ld64(rsrc_t r, u32 off, u32 so = 0) { return __builtin_amdgcn_raw_buffer_load_b64(r, off, so, 0); }
DI v2u ld64c(rsrc_t r, u32 off, u32 so = 0) { return __builtin_amdgcn_raw_buffer_load_b64(r, off, so, 16); }
DI void st128c(rsrc_t r, u32 off, u32 so, v4u v) { __builtin_amdgcn_raw_buffer_store_b128(v, r, off, so, 16); }
DI void st64c(rsrc_t r, u32 off, u32 so, v2u v) { __builtin_amdgcn_raw_buffer_store_b64(v, r, off, so, 16); }
DI void st32c(rsrc_t r, u32 off, u32 so, u32 v) { __builtin_amdgcn_raw_buffer_store_b32(v, r, off, so, 16); }

struct Lds {
  LAS unsigned char* p;
  DI v4u r128(u32 o) const { return *(LAS v4u*)(p + o); }
  DI v2u r64(u32 o) const { return *(LAS v2u*)(p + o); }
  DI f32x4 rf4(u32 o) const { return *(LAS f32x4*)(p + o); }
  DI f32x2 rf2(u32 o) const { return *(LAS f32x2*)(p + o); }
  DI float rf(u32 o) const { return *(LAS float*)(p + o); }
  DI void w128(u32 o, v4u v) const { *(LAS v4u*)(p + o) = v; }
  DI void w64(u32 o, v2u v) const { *(LAS v2u*)(p + o) = v; }
  DI void wf4(u32 o, f32x4 v) const { *(LAS f32x4*)(p + o) = v; }
  DI void wf2(u32 o, f32x2 v) const { *(LAS f32x2*)(p + o) = v; }
  DI void wf(u32 o, float v) const { *(LAS float*)(p + o) = v; }
  DI v2u tr(u32 o) const { return __builtin_bit_cast(v2u, __builtin_amdgcn_ds_read_tr16_b64_v4i16((LAS s16x4*)(p + o))); }
  DI v4u tr2(u32 o0, u32 o1) const { v2u a = tr(o0), b = tr(o1); v4u r = {a.x, a.y, b.x, b.y}; return r; }
};

DI void wait_flags(rsrc_t ws, u32 off, bool active, u32 tlo, u32 thi, u32 code) {
  for (int spins = 0;; ++spins) {
    asm volatile("" ::: "memory");
    v2u v = ld64c(ws, off);
    bool ok = !active || (v.x == tlo && v.y == thi);
    if (__all(ok)) break;
    if (spins > SPIN_CAP) { st32c(ws, WS_TMO, 0, code); break; }
    __builtin_amdgcn_s_sleep(1);
  }
  asm volatile("" ::: "memory");
}
#define PSL 4
DI void wait_flags_p(rsrc_t ws, u32 off, u32 tlo, u32 thi, u32 code) {
  const u64 tag = ((u64)thi << 32) | tlo;
  u32 left;
  asm volatile(
      "s_mov_b32 %0, %4\n\t"
      "buffer_load_dwordx2 v[250:251], %1, %2, 0 offen sc1\n\t"
      "s_waitcnt vmcnt(0)\n\t"
      "v_cmp_eq_u64 vcc, %3, v[250:251]\n\ts_cmp_eq_u64 vcc, exec\n\ts_cbranch_scc1 2f\n\t"
      "buffer_load_dwordx2 v[250:251], %1, %2, 0 offen sc1\n\ts_sleep %5\n\t"
      "buffer_load_dwordx2 v[252:253], %1, %2, 0 offen sc1\n\ts_sleep %5\n\t"
      "buffer_load_dwordx2 v[254:255], %1, %2, 0 offen sc1\n"
      "1:\n\t"
      "s_waitcnt vmcnt(2)\n\t"
      "v_cmp_eq_u64 vcc, %3, v[250:251]\n\ts_cmp_eq_u64 vcc, exec\n\ts_cbranch_scc1 2f\n\t"
      "buffer_load_dwordx2 v[250:251], %1, %2, 0 offen sc1\n\t"
      "s_waitcnt vmcnt(2)\n\t"
      "v_cmp_eq_u64 vcc, %3, v[252:253]\n\ts_cmp_eq_u64 vcc, exec\n\ts_cbranch_scc1 2f\n\t"
      "buffer_load_dwordx2 v[252:253], %1, %2, 0 offen sc1\n\t"
      "s_waitcnt vmcnt(2)\n\t"
      "v_cmp_eq_u64 vcc, %3, v[254:255]\n\ts_cmp_eq_u64 vcc, exec\n\ts_cbranch_scc1 2f\n\t"
      "buffer_load_dwordx2 v[254:255], %1, %2, 0 offen sc1\n\t"
      "s_sub_u32 %0, %0, 1\n\ts_cmp_lg_u32 %0, 0\n\ts_cbranch_scc1 1b\n"
      "2:"
      : "=&s"(left) : "v"(off), "s"(ws), "s"(tag), "n"(SPIN_CAP / 3), "n"(PSL)
      : "v250", "v251", "v252", "v253", "v254", "v255", "vcc", "scc", "memory");
  if (left == 0) st32c(ws, WS_TMO, 0, code);
}
template <class T> DI void tie(T& x) { asm volatile("" : "+v"(x)); }
template <int N, class T> DI void tiew(T& x) { asm volatile("s_waitcnt vmcnt(%1)" : "+v"(x) : "n"(N) : "memory"); }
DI void set_flag(rsrc_t ws, u32 idx, u32 tlo, u32 thi) { v2u t = {tlo, thi}; st64c(ws, WS_FLAG + idx * 256, 0, t); }
DI bool gran_ok(v4u a, u32 glo, u32 ghi) { return (a.x & 0xffff0000u) == glo && a.y == ghi && (a.z & 0xffff0000u) == glo && a.w == ghi; }

DI void feature_map(const f32x16& pin, int h, u32 (&P1)[8], u32 (&P2)[8]) {
  const float L2E = 1.4426950408889634f, INF = __builtin_inff();
  float pm[16], pn[16];
#pragma unroll
  for (int r = 0; r < 16; ++r) { pm[r] = pin[r]; pn[r] = pin[r]; }
#pragma unroll
  for (int r = 12; r < 16; ++r) { pm[r] = h ? -INF : pin[r]; pn[r] = h ? INF : pin[r]; }
  float mx = fmaxf(pm[0], pm[1]), mn = fminf(pn[0], pn[1]);
#pragma unroll
  for (int r = 2; r < 16; r += 2) { mx = fmaxf(fmaxf(mx, pm[r]), pm[r + 1]); mn = fminf(fminf(mn, pn[r]), pn[r + 1]); }
  mx = half_max(mx); mn = half_min(mn);
  const f32x2 cmx = bc(-mx * L2E), cmn = bc(mn * L2E), pl = bc(L2E), nl = bc(-L2E);
  f32x2 e1[8], e2[8];
#pragma unroll
  for (int i = 0; i < 8; ++i) {
    const f32x2 a = {pm[2 * i], pm[2 * i + 1]}, b = {pn[2 * i], pn[2 * i + 1]};
    const f32x2 ta = fma2(a, pl, cmx), tb = fma2(b, nl, cmn);
    e1[i] = f32x2{__builtin_amdgcn_exp2f(ta.x), __builtin_amdgcn_exp2f(ta.y)};
    e2[i] = f32x2{__builtin_amdgcn_exp2f(tb.x), __builtin_amdgcn_exp2f(tb.y)};
  }
  f32x2 s1 = ((e1[0] + e1[1]) + (e1[2] + e1[3])) + ((e1[4] + e1[5]) + (e1[6] + e1[7]));
  f32x2 s2 = ((e2[0] + e2[1]) + (e2[2] + e2[3])) + ((e2[4] + e2[5]) + (e2[6] + e2[7]));
  const f32x2 r1 = bc(__builtin_amdgcn_rcpf(half_sum(s1.x + s1.y))), r2 = bc(__builtin_amdgcn_rcpf(half_sum(s2.x + s2.y)));
#pragma unroll
  for (int i = 0; i < 8; ++i) { P1[i] = pkv(e1[i] * r1); P2[i] = pkv(e2[i] * r2); }
}

constexpr int NB0 = 12;
constexpr int SST = 8;
constexpr int AW = 12;
constexpr int PA_I = 12, PA_C = 15;
constexpr int PB_I = 10, PB_C = 15;
struct Plan { int wS, wB[28], wBar[28], wPollA, wAct[56], wPollB; };
constexpr int clampv(int v) { return v > 63 ? 63 : v; }
constexpr Plan mk_plan() {
  Plan P{};
  int n = 0, iB[28] = {}, iA[56] = {}, iStore = 0, iPoll = 0;
  n += 7; const int iS = n - 1;
  for (int s = 0; s < NB0; ++s) iB[s] = n++;
  for (int G = 0; G < 2; ++G) {
    for (int s = 0; s < 28; ++s) {
      const int pos = G * 7 + s / 4;
      if (G == 0) P.wB[s] = clampv(n - 1 - iB[s]);
      if (G == 0 && s == SST) P.wS = clampv(n - 1 - iS);
      if (G == 1 && s == PA_I) iPoll = n - 1;
      if (G == 0 && s + NB0 < 28) iB[s + NB0] = n++;
      if (G == 1 && s > PA_C) iA[s - PA_C - 1] = n++;
      if ((s & 3) == 3) P.wBar[pos] = pos == 8 ? clampv(n - 1 - iStore) : 63;
      if (G == 1 && s == PA_C) P.wPollA = clampv(n - 1 - iPoll);
    }
    n += 4; iStore = n - 1;
  }
  for (int o = 0; o < 56; ++o) {
    const int pos = 14 + o / 4;
    if (o == PB_I) iPoll = n - 1;
    P.wAct[o] = clampv(n - 1 - iA[o]);
    if (o + AW < 56) iA[o + AW] = n++;
    if ((o & 3) == 3) P.wBar[pos] = pos == 14 ? clampv(n - 1 - iStore) : 63;
    if (o == PB_C) P.wPollB = clampv(n - 1 - iPoll);
  }
  return P;
}
constexpr Plan PL = mk_plan();
static_assert(27 - PA_C == AW && (PA_C & 3) == 3 && (PB_C & 3) == 3 && PB_C + 1 + AW == 28, "plan");

DI float silu_f(float c) { return c * __builtin_amdgcn_rcpf(1.f + __builtin_amdgcn_exp2f(-1.4426950408889634f * c)); }
DI u32 cmask(u32 v, u64 m) { u32 r; asm("v_cndmask_b32_e64 %0, 0, %1, %2" : "=v"(r) : "v"(v), "s"(m)); return r; }
DI u32 opq(u32 v) { asm("" : "+v"(v)); return v; }
DI void split2(float v, u32& hi, u32& lo) { hi = pk2(v, v) & 0xffffu; lo = pk2(v - bflo(hi), 0.f) & 0xffffu; }
#define SBAR() asm volatile("s_barrier" ::: "memory")

__global__ void __launch_bounds__(512, 2)
fused_block(const u16* __restrict__ gx_, const u16* __restrict__ gwqkv_, const u16* __restrict__ gwphi_, const u16* __restrict__ gwo_,
            const u16* __restrict__ gwg_, const u16* __restrict__ gwu_, const u16* __restrict__ gwd_, const u16* __restrict__ gc_,
            const u16* __restrict__ gwmod_, u16* __restrict__ gout_, unsigned char* __restrict__ gws_) {
  extern __shared__ __attribute__((aligned(16))) unsigned char smem[];
  Lds L; L.p = (LAS unsigned char*)smem;
  const u32 lds0 = (u32)(uintptr_t)L.p;
  int tid = threadIdx.x, lane = tid & 63;
  const int w = __builtin_amdgcn_readfirstlane(tid >> 6);
  const int bid = blockIdx.x, xq = bid & 7, iq = bid >> 3;
  {
    u64 pc; u32 a_, b_, o_;
    asm volatile("s_getpc_b64 %0\n.Lfb_code0:\n\tv_readfirstlane_b32 %2, %4\n\ts_cmp_lg_u32 %2, 256\n\ts_cbranch_scc1 1f\n\t"
                 "s_mov_b32 %1, .Lfb_code1-.Lfb_code0\n\ts_max_u32 %1, %1, .Lfb_code2-.Lfb_code0\n\ts_add_i32 %1, %1, -4\n\t"
                 "s_lshr_b32 %2, %5, 3\n\ts_lshl_b32 %2, %2, 6\n\tv_and_b32 %3, 63, %4\n\tv_add_u32 %3, %2, %3\n\tv_lshlrev_b32 %3, 6, %3\n\t"
                 "v_min_u32 %3, %1, %3\n\tglobal_load_dword v255, %3, %0\n1:"
                 : "=&s"(pc), "=&s"(a_), "=&s"(b_), "=&v"(o_) : "v"(threadIdx.x), "s"(blockIdx.x) : "v255", "scc", "memory");
  }
  const u32 gmask = (xq & 1) ? 0xD5D5D5D5u : 0xABABABABu;
  int rank = ((gmask >> iq) & 1u) ? __builtin_popcount(gmask & ((1u << iq) - 1u)) : 99;
  int chunkC = (rank >> 1) * 16 + 2 * xq + (rank & 1);
  u32 vc0 = (tid >> 2) * 16, vo0 = (tid >> 2) * 8 * 5376 + chunkC * 64 + (tid & 3) * 16;
  asm volatile("" : "+s"(chunkC), "+s"(rank), "+v"(vc0), "+v"(vo0));
  const int chunkN = chunkC - 4;
  const bool p0 = rank < 4 && chunkC < 28, p0n = rank >= 4 && rank < 12 && chunkN < 84;
  u64 a0_ = (u64)gx_, a1_ = (u64)gwqkv_, a2_ = (u64)gwphi_, a3_ = (u64)gwo_, a4_ = (u64)gwg_, a5_ = (u64)gwu_, a6_ = (u64)gwd_, a7_ = (u64)gc_,
      a8_ = (u64)gwmod_, a9_ = (u64)gout_, aa_ = (u64)gws_;
  asm volatile("" : "+s"(a0_), "+s"(a1_), "+s"(a2_), "+s"(a3_), "+s"(a4_), "+s"(a5_), "+s"(a6_), "+s"(a7_), "+s"(a8_), "+s"(a9_), "+s"(aa_));
  const void *gx = (const void*)a0_, *gwqkv = (const void*)a1_, *gwphi = (const void*)a2_, *gwo = (const void*)a3_, *gwg = (const void*)a4_,
             *gwu = (const void*)a5_, *gwd = (const void*)a6_, *gc = (const void*)a7_, *gwmod = (const void*)a8_, *gout = (const void*)a9_, *gws = (const void*)aa_;
  int h, t32, g16, q4, p4;
#define RELANE() do { asm volatile("" : "+v"(tid)); lane = tid & 63; h = lane >> 5; t32 = lane & 31; g16 = (lane >> 4) & 1; q4 = (lane & 15) >> 2; p4 = lane & 3; } while (0)
  RELANE();

  const rsrc_t rWS = mk(gws, WS_USED);
  const rsrc_t rX = mk(gx, 2048 * 896), rWQKV = mk(gwqkv, 448 * 2688), rWPHI = mk(gwphi, 16 * 784 * 2), rWO = mk(gwo, 448 * 896);
  const rsrc_t rWG = mk(gwg, 448 * 3584), rWU = mk(gwu, 448 * 3584), rWD = mk(gwd, 1792 * 896), rC = mk(gc, 896), rWM = mk(gwmod, 448 * 5376);
  const rsrc_t rOUT = mk(gout, 2048 * 896);

  const int hd = 8 * (xq & 1) + (iq & 7), tt = 4 * (xq >> 1) + (iq >> 3);
  const bool p24 = iq < 28;
  const int t64 = 4 * xq + iq / 7, cs = iq % 7;
  const int tt3 = 4 * (xq >> 1) + iq / 7, fs3 = 7 * (xq & 1) + cs;
  const u32 zero = 0;

  const u32 so_mod = WS_MODG + xq * MODG_REP;
  u32 tlo, thi, glo, ghi;
  auto get_tag = [&](const v2u& e) {
    const u64 E = ((u64)__builtin_amdgcn_readfirstlane(e.y) << 32) | (u32)__builtin_amdgcn_readfirstlane(e.x);
    const u64 tag = E + 1;
    tlo = (u32)tag; thi = (u32)(tag >> 32);
    glo = tlo << 16; ghi = (u32)(tag >> 16);
  };
  auto silu2 = [&](u32 c2) -> u32 { return pk2(silu_f(bflo(c2)), silu_f(bfhi(c2))); };
  auto p0_part = [&](const v4u& sp, const v4u (&wm)[8], int wv) {
    float acc[8] = {};
#pragma unroll
    for (int jp = 0; jp < 4; ++jp)
#pragma unroll
      for (int e = 0; e < 4; ++e) {
        acc[2 * e] = dot2(__builtin_amdgcn_perm(wm[2 * jp + 1][e], wm[2 * jp][e], 0x05040100u), sp[jp], acc[2 * e]);
        acc[2 * e + 1] = dot2(__builtin_amdgcn_perm(wm[2 * jp + 1][e], wm[2 * jp][e], 0x07060302u), sp[jp], acc[2 * e + 1]);
      }
    float r[8];
#pragma unroll
    for (int j = 0; j < 8; ++j) r[j] = dpp_add<ROR8>(dpp_add<ROR4>(acc[j]));
    if ((lane & 12) == 0) {
      const u32 base = L_P0S + (wv * 4 + (lane >> 4)) * 128 + (lane & 3) * 32;
      f32x4 a = {r[0], r[1], r[2], r[3]}, b = {r[4], r[5], r[6], r[7]};
      L.wf4(base, a); L.wf4(base + 16, b);
    }
  };
  auto p0_publish = [&](int chunk, int wv) {
    const int rep = wv * 4 + (lane >> 4), pr_ = lane & 15;
    f32x2 v = {0.f, 0.f};
#pragma unroll
    for (int gI = 0; gI < 16; ++gI) v += L.rf2(L_P0S + gI * 128 + pr_ * 8);
    float v0 = rbf(v.x), v1 = rbf(v.y);
    if ((chunk >= 14 && chunk < 28) || (chunk >= 56 && chunk < 70)) { v0 = 1.f + v0; v1 = 1.f + v1; }
    const u32 pk = pk2(v0, v1);
    v4u gr = {glo | (pk & 0xffffu), ghi, glo | (pk >> 16), ghi};
    st128c(rWS, rep * MODG_REP + 2 * pr_ * 8, WS_MODG + chunk * 256, gr);
  };
  v2u ep;
  v4u cw, wm[8];
  if (w < 4) {
    ald64c(ep, zero, rWS, zero);
    if (p0) {
      ald128<0>(cw, vc0, rC, zero);
      const u32 vo = vo0;
      sfor<0, 8>([&](auto j) { ald128<0>(wm[j.value], vo, rWM, j.value * 5376); });
    }
  }
  if (LEXP(w >= 4)) {
    const int wl = w - 4;
    const u32 rr = lane >> 3;
    const u32 vW3 = rr * 3584 + fs3 * 256 + ((((lane & 7) >> 1) ^ ((rr >> 1) & 1) ^ (wl & 2)) * 32) + (lane & 1) * 16;
    const u32 vD = rr * 896 + cs * 128 + (((lane & 7) ^ (((rr >> 1) & 1) << 2)) * 16);
    const u32 sg3 = (wl & 1) * 32 * 3584, sgD = ((wl >> 1) * 896 + (wl & 1) * 32) * 896, slR = lds0 + wl * 4096;
    const rsrc_t rW3 = wl < 2 ? rWG : rWU;
    auto ring = [&](auto nc_) {
      constexpr int n = nc_.value;
      if constexpr (n < 14) dma<4, (n % 9) * SLOT, 1024, (64 * (n % 7)) * 3584 + (n / 7) * 128, 8 * 3584>(vW3, rW3, sg3, slR);
      else dma<4, (n % 9) * SLOT, 1024, (((n - 14) % 7) * 128 + ((n - 14) / 7) * 64) * 896, 8 * 896>(vD, rWD, sgD, slR);
    };
    u32 cpf = 0;
    v2u epl;
    ald64c(epl, zero, rWS, zero);
    const u32 tl = tid - 256;
    {
      if (wl == 3) ald32(cpf, min((u32)(bid * 37 + min(lane, 36)), 9407u) * 128u, rWQKV, 0u);
    }
    if (lane < 56) {
      const u32 sg = (tt * 128 + wl * 32) * 896, sl = lds0 + L_XS + wl * 28672;
      sfor<0, 8>([&](auto k) { dma_x4<2 * k.value>((lane * 16) ^ (k.value * 16), rX, sg, sl); });
    }
    const u32 vW = (lane >> 2) * 2688 + hd * 56 + min((lane & 3) * 16, 40);
    const u32 sgW = wl * 43008;
    auto wkv1 = [&]() {
      sfor<0, 8>([&](auto pc) {
        constexpr int n = pc.value >> 1, m = pc.value & 1;
        dma<1, L_WKV1 + n * 8192 + m * 1024, 0, n * 4 * 43008 + 896 + m * 896, 0>(vW, rWQKV, sgW, lds0 + wl * 2048);
      });
    };
    const bool early = !p0;
    if (early) wkv1();
    if (p0) SBAR();
    if (early) VMW(8); else VMW(0);
    asm volatile("" : "+v"(cpf), "+v"(epl)::"memory");
    get_tag(epl);
    SBAR();
    if (!early) wkv1();
    {
      const bool act = tl < 224;
      v4u ga_, gb_;
      for (int spins = 0;; ++spins) {
        ald128c<0>(ga_, tl * 16, rWS, so_mod); ald128c<448 * 8>(gb_, tl * 16, rWS, so_mod);
        asm volatile("s_waitcnt vmcnt(0)" : "+v"(ga_), "+v"(gb_)::"memory");
        const bool ok = !act || (gran_ok(ga_, glo, ghi) && gran_ok(gb_, glo, ghi));
        if (__all(ok)) break;
        if (spins > SPIN_CAP) { st32c(rWS, WS_TMO, 0, 1); break; }
      }
      if (act) {
        const u32 g0 = gb_.x & 0xffffu, g1 = gb_.z << 16, s0 = (ga_.x & 0xffffu) * 0x10001u, s1 = (ga_.z & 0xffffu) * 0x10001u;
        L.w128(L_T1 + tl * 32, v4u{g0, g0, s0, s0}); L.w128(L_T1 + tl * 32 + 16, v4u{g1, g1, s1, s1});
      }
    }
    VMW(0);
    BAR();
    sfor<0, 6>([&](auto pc) {
      constexpr int n = 4 + (pc.value >> 1), m = pc.value & 1;
      dma<1, L_WKV2 + (n - 4) * 8192 + m * 1024, 0, n * 4 * 43008 + 896 + m * 896, 0>(vW, rWQKV, sgW, lds0 + wl * 2048);
    });
    dma<7, L_WQ, 4096, 0, 4 * 43008>(vW, rWQKV, sgW, lds0 + wl * 1024);
    VMW(7);
    SBAR();
    {
      const rsrc_t rPF0 = wl == 2 ? rWG : rWD, rPF1 = wl == 2 ? rWU : rWO;
      const u32 vPF0 = wl >= 2 ? (bid * 49 + min(lane, 48)) * 128 : 0x7ffffff0;
      const u32 vPF1 = wl == 2 ? vPF0 : (wl == 3 ? min(bid * 13 + min(lane, 12), 3135) * 128 : 0x7ffffff0);
      warm(vPF0, rPF0, zero, lds0 + wl * 1024);
      warm(vPF1, rPF1, zero, lds0 + wl * 1024);
    }
    v4u wmN[8];
    {
      const u32 voN = p0n ? (tl >> 2) * 8 * 5376 + chunkN * 64 + (tl & 3) * 16 : 0x7ff00000;
      sfor<0, 8>([&](auto j) { ald128<0>(wmN[j.value], voN, rWM, j.value * 5376); });
    }
    VMW(10);
    SBAR();
    {
      const u32 vO = rr * 896 + cs * 128 + (((lane & 7) ^ (((rr >> 1) & 1) << 2)) * 16), sgO = wl * 7168;
      const u32 vR = rr * 896 + cs * 128 + (((lane & 7) ^ (rr >> 1)) * 16), sgR = (t64 * 64 + wl * 16) * 896;
      dma<14, L_WO, 4096, 0, 28672>(vO, rWO, sgO, lds0 + wl * 1024);
      dma<1, L_XR, 0, 0, 0>(vR, rX, sgR, lds0 + wl * 2048);
      dma<1, L_XR + 1024, 0, 8 * 896, 0>(vR ^ 64, rX, sgR, lds0 + wl * 2048);
    }
    SBAR();
    asm volatile("s_waitcnt vmcnt(16)" : "+v"(wmN[0]), "+v"(wmN[1]), "+v"(wmN[2]), "+v"(wmN[3]), "+v"(wmN[4]), "+v"(wmN[5]), "+v"(wmN[6]), "+v"(wmN[7])::"memory");
    if (p0n) p0_part(L.r128(L_SIL + (tl >> 2) * 16), wmN, wl);
    wait_flags_p(rWS, WS_FLAG + (FL_KV + hd * 16 + (lane & 15)) * 256, tlo, thi, 3);
    {
      v4u pp[16];
      const u32 so = WS_KVP + hd * 16 * 4096 + wl * 1024;
      sfor<0, 16>([&](auto i) { ald128c<0>(pp[i.value], lane * 16, rWS, so + i.value * 4096); });
      u32 sel[4];
#pragma unroll
      for (int j = 0; j < 4; ++j) {
        const int r0 = 8 * (j >> 1) + 4 * (lane >> 5) + 2 * (j & 1);
        sel[j] = ((lane & 31) == r0 ? 0x3f80u : 0u) | ((lane & 31) == r0 + 1 ? 0x3f800000u : 0u);
      }
      const v4u A = {sel[0], sel[1], sel[2], sel[3]};
      f32x16 d0 = {}, d1 = {};
      sfor<0, 8>([&](auto i) {
        tiew<15 - 2 * i.value>(pp[2 * i.value]);
        d0 = mfma(A, pp[2 * i.value], d0);
        SCHED();
        tiew<14 - 2 * i.value>(pp[2 * i.value + 1]);
        d1 = mfma(A, pp[2 * i.value + 1], d1);
        SCHED();
      });
      const f32x16 d = d0 + d1;
      v4u o = {pkv(pr<0>(d)), pkv(pr<2>(d)), pkv(pr<4>(d)), pkv(pr<6>(d))};
      L.w128(L_KVR + wl * 1024 + lane * 16, o);
    }
    BAR();
    if (p0n && wl < 2) p0_publish(chunkN, wl);
    if (p24) { ring(IC<0>{}); ring(IC<1>{}); ring(IC<2>{}); ring(IC<3>{}); }
    SBAR();
    if (!p24) return;
    SBAR();
    if (wl < 3) {
      const u32 vtg = ((wl == 0 ? 896 : (wl == 1 ? 1792 : 2240)) + cs * 64 + lane) * 8;
      v2u mtg;
      for (int spins = 0;; ++spins) {
        asm volatile("" ::: "memory");
        mtg = ld64c(rWS, vtg, so_mod);
        if (__all((mtg.x & 0xffff0000u) == glo && mtg.y == ghi)) break;
        if (spins > SPIN_CAP) { st32c(rWS, WS_TMO, 0, 2); break; }
      }
      L.wf(L_TGA + tl * 4, bflo(mtg.x));
    }
    BAR();
    ring(IC<4>{}); ring(IC<5>{}); ring(IC<6>{});
    SBAR();
    ring(IC<7>{}); ring(IC<8>{});
    VMW(28);
    SBAR();
    dma<1, L_EXT, 0, WS_EXT, 0>(lane * 16, rWS, fs3 * 4096 + wl * 1024, lds0 + wl * 1024);
    sfor<0, 26>([&](auto pc) {
      constexpr int pos = pc.value;
      if constexpr (pos >= 1 && pos + 8 <= 27) ring(IC<pos + 8>{});
      constexpr int last = pos + 8 <= 27 ? (pos >= 1 ? pos + 8 : 8) : 27;
      constexpr int yng = 4 * (last - (pos + 2)) + (pos + 2 <= 8 ? 1 : 0);
      constexpr int wv = pos == 5 ? (yng < 20 ? yng : 20) : yng;
      VMW(wv);
      SBAR();
    });
    asm volatile(".Lfb_code2:");
    return;
  }
  if (p0) {
    asm volatile("s_waitcnt vmcnt(8)" : "+v"(ep), "+v"(cw)::"memory");
    get_tag(ep);
    v4u sp = {silu2(cw.x), silu2(cw.y), silu2(cw.z), silu2(cw.w)};
    tie(sp);
    asm volatile("s_waitcnt vmcnt(0)" : "+v"(wm[0]), "+v"(wm[1]), "+v"(wm[2]), "+v"(wm[3]), "+v"(wm[4]), "+v"(wm[5]), "+v"(wm[6]), "+v"(wm[7])::"memory");
    p0_part(sp, wm, w);
    BAR();
    if (w < 2) p0_publish(chunkC, w);
  }
  v2u wph;
  ald64(wph, ((tid >> 3) < 28 && (tid & 7) < 7) ? (hd * 784 + (tid >> 3) * 28 + (tid & 7) * 4) * 2 : 0x7ffffff0, rWPHI, zero);
  u32 c2;
  ald32(c2, tid < 224 ? tid * 4 : 0x7ffffff0, rC, zero);

  asm volatile("s_waitcnt vmcnt(2)" : "+v"(ep)::"memory");
  if (!p0) get_tag(ep);
  SBAR();
  v4u X[28];
  {
    const u32 gsw = (t32 >> 1) & 7;
    u32 xb[4];
#pragma unroll
    for (int j = 0; j < 4; ++j) xb[j] = L_XS + w * 28672 + t32 * 896 + (((2 * j + h) ^ gsw) * 16);
#pragma unroll
    for (int s = 0; s < 28; ++s) X[s] = L.r128(xb[s & 3] + (s >> 2) * 128);
  }
  float rstd1;
  v4u Bc;
  {
    float s0 = 0.f, s1 = 0.f, q0 = 0.f, q1 = 0.f;
    sfor<0, 28>([&](auto sc) {
      constexpr int s = sc.value;
      s0 = dot2(X[s][0], 0x3f803f80u, s0); s1 = dot2(X[s][1], 0x3f803f80u, s1);
      s0 = dot2(X[s][2], 0x3f803f80u, s0); s1 = dot2(X[s][3], 0x3f803f80u, s1);
      q0 = dot2(X[s][0], X[s][0], q0); q1 = dot2(X[s][1], X[s][1], q1);
      q0 = dot2(X[s][2], X[s][2], q0); q1 = dot2(X[s][3], X[s][3], q1);
      if constexpr (s % 4 == 3) SCHED();
    });
    const float m = half_sum(s0 + s1) * (1.f / 448.f);
    const float var = fmaxf(half_sum(q0 + q1) * (1.f / 448.f) - m * m, 0.f) + 1e-5f;
    rstd1 = __builtin_amdgcn_rsqf(var);
    u32 mh, ml, sh, sl_;
    split2(-m, mh, ml); split2(var * rstd1, sh, sl_);
    Bc = h ? v4u{0u, 0u, 0u, 0u} : v4u{mh | (mh << 16), ml | (ml << 16), sh | (sl_ << 16), 0u};
    if (h == 0) L.wf(L_ST1 + (w * 32 + t32) * 4, rstd1);
  }
  asm volatile("s_waitcnt vmcnt(0)" : "+v"(wph), "+v"(c2)::"memory");
  L.w64(L_WPH + (tid >> 3) * 64 + (tid & 7) * 8, wph);
  if (p0n) *(LAS u32*)(L.p + L_SIL + tid * 4) = silu2(c2);
  u64 m1[4], m2[4];
#pragma unroll
  for (int d = 0; d < 4; ++d) {
    m1[d] = (3ull << (2 * d)) | (3ull << (40 + 2 * d)); m2[d] = (3ull << (16 + 2 * d)) | (3ull << (56 + 2 * d));
    asm volatile("" : "+s"(m1[d]), "+s"(m2[d]));
  }
  RELANE();
  const u32 coff = g16 * 32 + ((p4 ^ (g16 & (p4 >> 1))) * 8);
  const u32 tb = opq(L_T1 + t32 * 16);
  const u32 wb1 = opq(L_WKV1 + (4 * h + q4) * 64 + coff), wb2 = opq(L_WKV2 + (4 * h + q4) * 64 + coff);
  BAR();

  f32x16 accK = {}, accV = {};
  v4u H[28];
  {
    auto rdW = [&](int s, int mat) -> v4u { const u32 b = s < 16 ? wb1 + s * 2048 + mat * 1024 : wb2 + (s - 16) * 2048 + mat * 1024; return L.tr2(b, b + 512); };
    auto mkA = [&](const v4u& ac, const u64 (&m)[4]) -> v4u { v4u a = {cmask(ac.x, m[0]), cmask(ac.x, m[1]), cmask(ac.x, m[2]), cmask(ac.x, m[3])}; return a; };
    const f32x16 Z = {};
    f32x16 D[2];
    v4u Ac[2], A1, A2, fk[2], fv[2];
    Ac[0] = L.r128(tb); Ac[1] = L.r128(tb + 512);
    const v4u Ac2 = L.r128(tb + 2 * 512);
    fk[0] = rdW(0, 0); fv[0] = rdW(0, 1); fk[1] = rdW(1, 0); fv[1] = rdW(1, 1);
    SCHED();
    D[0] = mfma(mkA(Ac[0], m1), X[0], Z);
    D[1] = mfma(mkA(Ac[1], m1), X[2], Z);
    D[0] = mfma(mkA(Ac[0], m2), X[1], D[0]);
    D[1] = mfma(mkA(Ac[1], m2), X[3], D[1]);
    D[0] = mfma(Ac[0], Bc, D[0]);
    D[1] = mfma(Ac[1], Bc, D[1]);
    Ac[0] = Ac2;
    A1 = mkA(Ac[0], m1);
    H[0] = v4u{pkv(pr<0>(D[0])), pkv(pr<2>(D[0])), pkv(pr<4>(D[0])), pkv(pr<6>(D[0]))};
    H[1] = v4u{pkv(pr<8>(D[0])), pkv(pr<10>(D[0])), pkv(pr<12>(D[0])), pkv(pr<14>(D[0]))};
    SCHED();
    sfor<0, 14>([&](auto pc_) {
      constexpr int p = pc_.value;
      constexpr bool nd = p + 2 < 14, nc = p + 1 < 14, n3 = p + 3 < 14;
      if constexpr (p == 7) SBAR();
      f32x16& Dn = D[p & 1];
      const f32x16& Dc = D[(p + 1) & 1];
      if constexpr (nd) { Dn = mfma(A1, X[2 * p + 4], Z); SCHED(); A2 = mkA(Ac[p & 1], m2); }
      SCHED();
      accK = mfma(fk[0], H[2 * p], accK);
      SCHED();
      if constexpr (nc) { fk[0] = rdW(2 * p + 2, 0); H[2 * p + 2].x = pkv(pr<0>(Dc)); H[2 * p + 2].y = pkv(pr<2>(Dc)); }
      SCHED();
      if constexpr (nd) Dn = mfma(A2, X[2 * p + 5], Dn);
      SCHED();
      if constexpr (n3) Ac[(p + 1) & 1] = L.r128(tb + (p + 3) * 512);
      if constexpr (nc) { H[2 * p + 2].z = pkv(pr<4>(Dc)); H[2 * p + 2].w = pkv(pr<6>(Dc)); H[2 * p + 3].x = pkv(pr<8>(Dc)); }
      SCHED();
      accV = mfma(H[2 * p], fv[0], accV);
      SCHED();
      if constexpr (nc) { fv[0] = rdW(2 * p + 2, 1); H[2 * p + 3].y = pkv(pr<10>(Dc)); H[2 * p + 3].z = pkv(pr<12>(Dc)); }
      SCHED();
      if constexpr (nd) Dn = mfma(Ac[p & 1], Bc, Dn);
      SCHED();
      if constexpr (n3) A1 = mkA(Ac[(p + 1) & 1], m1);
      SCHED();
      accK = mfma(fk[1], H[2 * p + 1], accK);
      SCHED();
      if constexpr (nc) { fk[1] = rdW(2 * p + 3, 0); H[2 * p + 3].w = pkv(pr<14>(Dc)); }
      SCHED();
      accV = mfma(H[2 * p + 1], fv[1], accV);
      SCHED();
      if constexpr (nc) fv[1] = rdW(2 * p + 3, 1);
      SCHED();
    });
  }
  v4u PH[2];
  {
    const u32 b = opq(L_WPH + (4 * h + q4) * 64 + g16 * 32 + p4 * 8);
    PH[0] = L.tr2(b, b + 8 * 64); PH[1] = L.tr2(b + 16 * 64, b + 24 * 64);
  }
  auto epi_t = [&](const f32x16& a, v4u& b0, v4u& b1) {
    const f32x2 r = bc(rstd1);
    b0 = v4u{pkv(pr<0>(a) * r), pkv(pr<2>(a) * r), pkv(pr<4>(a) * r), pkv(pr<6>(a) * r)};
    b1 = v4u{pkv(pr<8>(a) * r), pkv(pr<10>(a) * r), pkv(pr<12>(a) * r), pkv(pr<14>(a) * r)};
  };
  {
    v4u b0, b1;
    epi_t(accK, b0, b1);
    f32x16 pk = {};
    pk = mfma(PH[0], b0, pk); pk = mfma(PH[1], b1, pk);
    {
      const float one = t32 == 28 ? 1.f : 0.f;
      const u32 sb_ = opq(L_ST1 + (w * 32 + 4 * h) * 4);
      u32 vb[8];
#pragma unroll
      for (int qq = 0; qq < 4; ++qq) {
        const f32x4 r4 = L.rf4(sb_ + 32 * qq);
        float v[4];
#pragma unroll
        for (int e = 0; e < 4; ++e) { v[e] = accV[4 * qq + e] * r4[e]; v[e] = t32 < 28 ? v[e] : one; }
        vb[2 * qq] = pk2(v[0], v[1]); vb[2 * qq + 1] = pk2(v[2], v[3]);
      }
      v4u a = {vb[0], vb[1], vb[2], vb[3]}, b = {vb[4], vb[5], vb[6], vb[7]};
      L.w128(L_VFR + w * 2048 + lane * 16, a); L.w128(L_VFR + w * 2048 + 1024 + lane * 16, b);
    }
    u32 P1[8], P2[8];
    feature_map(pk, h, P1, P2);
    const u32 swt = (t32 & 7) | ((((t32 >> 3) ^ (t32 >> 1)) & 1) << 3);
    const u32 rb = L_PHI + w * 4096 + t32 * 128;
#pragma unroll
    for (int qq = 0; qq < 4; ++qq) {
      v2u a = {P1[2 * qq], P1[2 * qq + 1]}, b = {P2[2 * qq], P2[2 * qq + 1]};
      L.w64(rb + (((2 * qq + h) ^ swt) * 8), a);
      L.w64(rb + (((8 + 2 * qq + h) ^ swt) * 8), b);
    }
  }
  BAR();
  if (w < 2) {
    f32x16 kv = {};
    u32 ad[4];
#pragma unroll
    for (int su = 0; su < 4; ++su) {
      const u32 t = 8 * su + 4 * h + q4;
      const u32 sw_ = (t & 7) | ((((t >> 3) ^ (t >> 1)) & 1) << 3);
      ad[su] = L_PHI + t * 128 + (((8 * w + 4 * g16 + p4) ^ sw_) * 8);
    }
#pragma unroll
    for (int ww = 0; ww < 4; ++ww)
#pragma unroll
      for (int s = 0; s < 2; ++s) {
        const v4u a = L.tr2(ad[2 * s] + ww * 4096, ad[2 * s + 1] + ww * 4096);
        const v4u b = L.r128(L_VFR + ww * 2048 + s * 1024 + lane * 16);
        kv = mfma(a, b, kv);
      }
    v4u o0 = {pkv(pr<0>(kv)), pkv(pr<2>(kv)), pkv(pr<4>(kv)), pkv(pr<6>(kv))};
    v4u o1 = {pkv(pr<8>(kv)), pkv(pr<10>(kv)), pkv(pr<12>(kv)), pkv(pr<14>(kv))};
    const u32 so = WS_KVP + (hd * 16 + tt) * 4096 + w * 2048;
    st128c(rWS, lane * 16, so, o0); st128c(rWS, lane * 16 + 1024, so, o1);
  }
  RELANE();
  f32x16 accQ = {};
  {
    const u32 bq = opq(L_WQ + (4 * h + q4) * 64 + g16 * 32 + ((p4 ^ (g16 & (p4 >> 1))) * 8));
    v4u fq[4];
#pragma unroll
    for (int s = 0; s < 3; ++s) fq[s] = L.tr2(bq + s * 1024, bq + s * 1024 + 512);
    sfor<0, 28>([&](auto sc) {
      constexpr int s = sc.value;
      accQ = mfma(fq[s & 3], H[s], accQ);
      SCHED();
      if constexpr (s + 3 < 28) fq[(s + 3) & 3] = L.tr2(bq + (s + 3) * 1024, bq + (s + 3) * 1024 + 512);
      SCHED();
    });
  }
  if (w < 2) VMW(0);
  BAR();
  if (tid == 0) set_flag(rWS, FL_KV + hd * 16 + tt, tlo, thi);
  RELANE();
  u32 Q1[8], Q2[8];
  {
    v4u b0, b1;
    epi_t(accQ, b0, b1);
    f32x16 pq = {};
    pq = mfma(PH[0], b0, pq); pq = mfma(PH[1], b1, pq);
    SCHED();
    feature_map(pq, h, Q1, Q2);
  }
  BAR();
  RELANE();
  {
    f32x16 o = {};
    const u32 kb = opq(L_KVR + lane * 16);
    v4u b0 = {Q1[0], Q1[1], Q1[2], Q1[3]}, b1 = {Q1[4], Q1[5], Q1[6], Q1[7]}, b2 = {Q2[0], Q2[1], Q2[2], Q2[3]}, b3 = {Q2[4], Q2[5], Q2[6], Q2[7]};
    o = mfma(L.r128(kb), b0, o);
    o = mfma(L.r128(kb + 1024), b1, o);
    o = mfma(L.r128(kb + 2048), b2, o);
    o = mfma(L.r128(kb + 3072), b3, o);
    const float den = half_hi(o[12]) + 1e-6f;
    const float rd = __builtin_amdgcn_rcpf(den), rd2 = h ? 0.f : rd;
    const f32x2 r1 = bc(rd), r2 = bc(rd2);
    v4u o0 = {pkv(pr<0>(o) * r1), pkv(pr<2>(o) * r1), pkv(pr<4>(o) * r1), pkv(pr<6>(o) * r1)};
    v4u o1 = {pkv(pr<8>(o) * r1), pkv(pr<10>(o) * r1), pkv(pr<12>(o) * r2), pkv(pr<14>(o) * r2)};
    const u32 so = WS_OFR + ((tt * 4 + w) * 32 + hd * 2) * 1024;
    st128c(rWS, lane * 16, so, o0); st128c(rWS, lane * 16 + 1024, so, o1);
  }
  VMW(0);
  BAR();
  if (tid == 0) set_flag(rWS, FL_O + tt * 16 + hd, tlo, thi);
  RELANE();
  if (!p24) {
    const int rank = (iq - 28) * 4 + (xq >> 1);
    if (rank < 14) {
      const int mat = rank / 7, f3 = 7 * (xq & 1) + rank % 7;
      VMW(0);
      BAR();
      {
        const u32 rr = lane >> 3;
        const u32 v = rr * 3584 + f3 * 256 + (((lane & 7) ^ (((rr >> 1) & 1) << 2)) * 16);
        dma<28, L_CI, 2048, 0, 16 * 3584>(v, mat ? rWU : rWG, (w >> 1) * 128 + (w & 1) * 8 * 3584, lds0 + (w >> 1) * 57344 + (w & 1) * 1024);
      }
      {
        const bool act = tid < 224;
        v4u a, b;
        for (int spins = 0;; ++spins) {
          asm volatile("" ::: "memory");
          a = ld128c(rWS, tid * 16 + 1792 * 8, so_mod); b = ld128c(rWS, tid * 16 + 1344 * 8, so_mod);
          const bool ok = !act || (gran_ok(a, glo, ghi) && gran_ok(b, glo, ghi));
          if (__all(ok)) break;
          if (spins > SPIN_CAP) { st32c(rWS, WS_TMO, 0, 7); break; }
        }
        const u32 kk = (2 * tid) & 15;
        const u32 o = L_CT + (tid >> 3) * 64 + ((kk >> 2) & 1) * 16 + ((kk & 3) + 4 * (kk >> 3)) * 2;
        if (act) {
          *(LAS u32*)(L.p + o) = (a.x & 0xffffu) | (a.z << 16);
          *(LAS u32*)(L.p + o + 32) = (b.x & 0xffffu) | (b.z << 16);
        }
      }
      VMW(0);
      BAR();
      f32x16 acc = {};
      const u32 sw = ((q4 >> 1) & 1) << 2;
      const u32 cb = opq(L_CI + (w >> 1) * 57344 + (4 * h + q4) * 128 + (((4 * (w & 1) + 2 * g16 + (p4 >> 1)) ^ sw) * 16) + (p4 & 1) * 8);
      const u32 tb = opq(L_CT + (lane & 1) * 32 + h * 16);
#pragma unroll
      for (int s = 0; s < 28; ++s) acc = mfma(L.tr2(cb + s * 2048, cb + s * 2048 + 1024), L.r128(tb + s * 64), acc);
      if (t32 < 2) {
#pragma unroll
        for (int qq = 0; qq < 4; ++qq) {
          f32x4 v = {acc[4 * qq], acc[4 * qq + 1], acc[4 * qq + 2], acc[4 * qq + 3]};
          L.wf4(L_CB + t32 * 512 + (w * 32 + 8 * qq + 4 * h) * 4, v);
        }
      }
      BAR();
      if (tid < 128) {
        u32 ch, cl, bh, bl;
        split2(L.rf(L_CB + tid * 4), ch, cl); split2(L.rf(L_CB + 512 + tid * 4), bh, bl);
        v4u e = {ch | (cl << 16), ch | (bh << 16), bl | (bh << 16), 0u};
        st128c(rWS, (tid >> 4) * 512 + (tid & 15) * 16, WS_EXT + f3 * 4096 + mat * 256, e);
      }
      VMW(0);
      BAR();
      if (tid == 0) set_flag(rWS, FL_C + mat * 14 + f3, tlo, thi);
    }
    return;
  }

  u32 x1p[8];
  const int tt2 = w & 1, kh = w >> 1;
  const u32 T32 = t64 * 2 + tt2;
  {

    if (w == 0) wait_flags(rWS, WS_FLAG + (FL_O + (t64 >> 1) * 16 + (lane & 15)) * 256, lane < 16, tlo, thi, 4);
    BAR();
    if (bid == 0 && tid == 0) { v2u t = {tlo, thi}; st64c(rWS, WS_EPOCH, 0, t); }
    v4u O[16];
    {
      const u32 so = WS_OFR + (T32 * 32 + kh * 16) * 1024;
#pragma unroll
      for (int i = 0; i < 16; ++i) O[i] = ld128c(rWS, lane * 16 + (i & 3) * 1024, so + (i >> 2) * 4096);
    }
    f32x16 acc[2] = {};
    {
      const u32 sw = ((q4 >> 1) & 1) << 2;
      const u32 cb0 = (((2 * g16 + (p4 >> 1)) ^ sw) * 16) + (p4 & 1) * 8, cb1 = (((4 + 2 * g16 + (p4 >> 1)) ^ sw) * 16) + (p4 & 1) * 8;
      const u32 rb = L_WO + kh * 8 * 28 * 128 + q4 * 128;
      const u32 bh0 = opq(rb + 4 * h * 128 + cb0), bh1 = opq(rb + 4 * h * 128 + cb1), bn0 = opq(rb + cb0), bn1 = opq(rb + cb1);
      auto rd = [&](int i, int ct) -> v4u {
        const int hh = i >> 1, s = i & 1;
        const u32 o0 = (28 * hh + 16 * s) * 128, o1 = o0 + 8 * 128;
        const u32 b = ct ? bh1 : bh0, bn = ct ? bn1 : bn0;
        return L.tr2(b + o0, (s ? bn : b) + o1);
      };
      v4u f[3][2];
      f[0][0] = rd(0, 0); f[0][1] = rd(0, 1); f[1][0] = rd(1, 0); f[1][1] = rd(1, 1);
#pragma unroll
      for (int i = 0; i < 16; ++i) {
        acc[0] = mfma(f[i % 3][0], O[i], acc[0]);
        SCHED();
        if (i + 2 < 16) f[(i + 2) % 3][0] = rd(i + 2, 0);
        SCHED();
        acc[1] = mfma(f[i % 3][1], O[i], acc[1]);
        SCHED();
        if (i + 2 < 16) f[(i + 2) % 3][1] = rd(i + 2, 1);
        SCHED();
      }
    }
    {
      const f32x16 snd = kh ? acc[0] : acc[1];
      const u32 xb = opq(L_XCH + w * 4096 + lane * 16);
#pragma unroll
      for (int qq = 0; qq < 4; ++qq) { f32x4 v = {snd[4 * qq], snd[4 * qq + 1], snd[4 * qq + 2], snd[4 * qq + 3]}; L.wf4(xb + qq * 1024, v); }
    }
    BAR();
    {
      const f32x16 mine = kh ? acc[1] : acc[0];
      const u32 xb = opq(L_XCH + (w ^ 2) * 4096 + lane * 16);
      const u32 row = tt2 * 32 + t32, rsw = (row >> 1) & 7;
      const u32 xrb = opq(L_XR + row * 128 + 8 * h), tg = opq(L_TGA + (kh * 32 + 4 * h) * 4);
      f32x2 x1[8];
      u32 a2[8];
      f32x2 sum = {0.f, 0.f};
      sfor<0, 4>([&](auto qq) {
        constexpr int Q = qq.value;
        const f32x4 o = L.rf4(xb + Q * 1024);
        const v2u xr = L.r64(xrb + (((kh * 4 + Q) ^ rsw) * 16));
        const f32x4 ga4 = L.rf4(tg + 32 * Q), g24 = L.rf4(tg + 256 + 32 * Q);
        x1p[2 * Q] = pkv(fma2(lo2(ga4), pr<4 * Q>(mine) + lo2(o), unp(xr.x)));
        x1p[2 * Q + 1] = pkv(fma2(hi2(ga4), pr<4 * Q + 2>(mine) + hi2(o), unp(xr.y)));
        x1[2 * Q] = unp(x1p[2 * Q]); x1[2 * Q + 1] = unp(x1p[2 * Q + 1]);
        a2[2 * Q] = pkv(x1[2 * Q] * lo2(g24)); a2[2 * Q + 1] = pkv(x1[2 * Q + 1] * hi2(g24));
        sum += x1[2 * Q] + x1[2 * Q + 1];
      });
      v4u o0 = {a2[0], a2[1], a2[2], a2[3]}, o1 = {a2[4], a2[5], a2[6], a2[7]};
      const u32 so = WS_X1F + (T32 * 28 + cs * 4 + kh * 2) * 1024;
      st128c(rWS, lane * 16, so, o0); st128c(rWS, lane * 16 + 1024, so, o1);
      const float mean = half_sum(sum.x + sum.y) * (1.f / 32.f);
      f32x2 m2v = {0.f, 0.f};
#pragma unroll
      for (int r = 0; r < 8; ++r) { const f32x2 d = x1[r] - bc(mean); m2v = fma2(d, d, m2v); }
      const float m2 = half_sum(m2v.x + m2v.y);
      if (h == 0) { v2u st = {__float_as_uint(mean), __float_as_uint(m2)}; st64c(rWS, t32 * 8, WS_STAT + (T32 * 14 + cs * 2 + kh) * 256, st); }
    }
  }
  VMW(0);
  BAR();
  if (tid == 0) set_flag(rWS, FL_X + t64 * 7 + cs, tlo, thi);
  if (w == 0) {
    const u32 l16 = lane & 15;
    const u32 fo = l16 < 14 ? FL_X + (tt3 * 2 + l16 / 7) * 7 + l16 % 7 : FL_C + (l16 - 14) * 14 + fs3;
    wait_flags_p(rWS, WS_FLAG + fo * 256, tlo, thi, 5);
  }
  BAR();
  RELANE();
  v4u B[28];
  const u32 T32p = tt3 * 4 + w;
  v2u st[7];
  const u32 sx = WS_X1F + T32p * 28 * 1024, vx = lane * 16;
  {
    const u32 vs = t32 * 8 + h * 7 * 256, so = WS_STAT + T32p * 14 * 256;
    sfor<0, 7>([&](auto i) { ald64c(st[i.value], vs, rWS, so + i.value * 256); });
    sfor<0, NB0>([&](auto s) { ald128c<(s.value & 3) * 1024>(B[s.value], vx, rWS, sx + (s.value >> 2) * 4096); });
  }
  float rs2;
  v4u Bx;
  auto ln2_stats = [&]() {
    asm volatile("s_waitcnt vmcnt(%7)" : "+v"(st[0]), "+v"(st[1]), "+v"(st[2]), "+v"(st[3]), "+v"(st[4]), "+v"(st[5]), "+v"(st[6]) : "n"(PL.wS) : "memory");
    float mean = 0.f, m2 = 0.f;
#pragma unroll
    for (int i = 0; i < 7; ++i) mean += __uint_as_float(st[i].x);
    mean = half_sum(mean) * (1.f / 14.f);
#pragma unroll
    for (int i = 0; i < 7; ++i) { const float d = __uint_as_float(st[i].x) - mean; m2 += fmaf(32.f * d, d, __uint_as_float(st[i].y)); }
    const float var = half_sum(m2) * (1.f / 448.f) + 1e-5f;
    const float rstd2 = __builtin_amdgcn_rsqf(var);
    rs2 = rstd2 * rstd2;
    u32 mh, ml, sh, sl_;
    split2(-mean, mh, ml); split2(var * rstd2, sh, sl_);
    Bx = h ? v4u{0u, 0u, 0u, 0u} : v4u{mh | (mh << 16), ml | (sh << 16), sh | (sl_ << 16), 0u};
  };
  v4u A_[56];
  const u32 sa0 = WS_ACT + (T32 * 112 + kh * 56) * 1024;
  auto a_load = [&](auto jc) {
    constexpr int j = jc.value;
    ald128c<(j & 3) * 1024>(A_[j], vx, rWS, sa0 + ((j % 28) >> 2) * 8192 + (j / 28) * 4096);
  };
  const u32 vfl = WS_FLAG + (FL_A + ((t64 >> 1) * 14 + (lane < 14 ? lane : 0)) * 2) * 256;
  v2u fa;
  {
    const u32 so_act = WS_ACT + (T32p * 112 + fs3 * 8) * 1024;
    u32 bA[4], bo[4];
#pragma unroll
    for (int i = 0; i < 4; ++i) bo[i] = g16 * 8192 + (4 * h + q4) * 128 + ((i ^ ((q4 >> 1) & 1) ^ (g16 * 2)) * 32) + p4 * 8;
    const u32 eb = opq(L_EXT + t32 * 16);
    int curgrp = -1;
    (void)curgrp;
    v4u f[2][4];
    auto setbase = [&](auto gc_) {
#pragma unroll
      for (int i = 0; i < 4; ++i) bA[i] = opq(bo[i] + gc_.value * 65536);
    };
    auto rdf = [&](auto Gc, auto sc, int i) -> v4u {
      constexpr int G = Gc.value, s = sc.value;
      if constexpr (s == 28) return L.r128(eb + (G * 4 + i) * 512);
      else {
        constexpr int slot = (G * 7 + s / 4) % 9;
        const u32 b = bA[i] + (slot & 3) * SLOT + (s & 3) * 2048;
        return L.tr2(b, b + 1024);
      }
    };
    setbase(IC<0>{});
#pragma unroll
    for (int i = 0; i < 4; ++i) f[0][i] = rdf(IC<0>{}, IC<0>{}, i);
    sfor<0, 2>([&](auto Gc) {
      constexpr int G = Gc.value;
      f32x16 acc[4] = {};
      sfor<0, 29>([&](auto sc) {
        constexpr int s = sc.value, par = (G * 29 + s) & 1;
        constexpr int pos = G * 7 + (s < 28 ? s / 4 : 6);
        constexpr int nG = s + 1 < 29 ? G : G + 1, ns = s + 1 < 29 ? s + 1 : 0;
        constexpr bool nx = nG < 2;
        if constexpr (nx && ns < 28) {
          constexpr int sl_n = (nG * 7 + ns / 4) % 9, sl_p = (ns == 0) ? (G * 7 + 6) % 9 : (nG * 7 + (ns - 1) / 4) % 9;
          if constexpr ((sl_n >> 2) != (sl_p >> 2)) setbase(IC<(sl_n >> 2)>{});
        }
        if constexpr (G == 0 && s < 28) asm volatile("s_waitcnt vmcnt(%1)" : "+v"(B[s]) : "n"(PL.wB[s]) : "memory");
        if constexpr (G == 1 && s == PA_I) { if (w == 0) ald64c(fa, vfl, rWS, zero); }
#pragma unroll
        for (int i = 0; i < 4; ++i) {
          if constexpr (s < 28) acc[i] = mfma(f[par][i], B[s], acc[i]); else acc[i] = mfma(f[par][i], Bx, acc[i]);
          if constexpr (nx) f[par ^ 1][i] = rdf(IC<nG>{}, IC<ns>{}, i);
          if constexpr (G == 0 && s + NB0 < 28) { if (i == 2) ald128c<((s + NB0) & 3) * 1024>(B[s + NB0], vx, rWS, sx + ((s + NB0) >> 2) * 4096); }
          if constexpr (G == 1 && s > PA_C && s < 28) { if (i == 2) a_load(IC<s - PA_C - 1>{}); }
          if constexpr (G == 0 && s == SST) { if (i == 3) ln2_stats(); }
          SCHED();
        }
        if constexpr (s < 28 && (s & 3) == 3) {
          if constexpr (G == 1 && s == PA_C) {
            if (w == 0) {
              asm volatile("s_waitcnt vmcnt(%1)" : "+v"(fa) : "n"(PL.wPollA) : "memory");
              if (!__all(fa.x == tlo && fa.y == thi)) wait_flags(rWS, vfl, true, tlo, thi, 6);
            }
          }
          if constexpr (PL.wBar[pos] < 63) VMW(PL.wBar[pos]);
          SBAR();
          if constexpr (pos == 8) { if (tid == 0) set_flag(rWS, FL_A + (tt3 * 14 + fs3) * 2, tlo, thi); }
        }
      });
      const f32x2 r2 = bc(rs2);
#pragma unroll
      for (int i = 0; i < 4; ++i) {
        const f32x16& a = acc[i];
        v4u ov = {pkv(pr<0>(a) * pr<8>(a) * r2), pkv(pr<2>(a) * pr<10>(a) * r2), pkv(pr<4>(a) * pr<12>(a) * r2), pkv(pr<6>(a) * pr<14>(a) * r2)};
        st128c(rWS, lane * 16 + i * 1024, so_act + G * 4096, ov);
      }
    });
  }
  {
    f32x16 acc[2] = {};
    const u32 sw = ((q4 >> 1) & 1) << 2;
    const u32 rb = kh * 8192 + (4 * h + q4) * 128 + (p4 & 1) * 8;
    const u32 co0 = rb + (((2 * g16 + (p4 >> 1)) ^ sw) * 16), co1 = rb + (((4 + 2 * g16 + (p4 >> 1)) ^ sw) * 16);
    u32 c0, c1;
    auto setb = [&](auto gc_) { c0 = opq(co0 + gc_.value * 65536); c1 = opq(co1 + gc_.value * 65536); };
    auto rd = [&](auto oc, int ct) -> v4u {
      constexpr int o = oc.value, slot = (14 + o / 4) % 9;
      const u32 b = (ct ? c1 : c0) + (slot & 3) * SLOT + (o & 3) * 2048;
      return L.tr2(b, b + 1024);
    };
    setb(IC<((14 % 9) >> 2)>{});
    v4u f[3][2];
    f[0][0] = rd(IC<0>{}, 0); f[0][1] = rd(IC<0>{}, 1); f[1][0] = rd(IC<1>{}, 0); f[1][1] = rd(IC<1>{}, 1);
    v2u fb;
    sfor<0, 56>([&](auto oc) {
      constexpr int o = oc.value, pos = 14 + o / 4;
      if constexpr (o + 2 < 56) {
        constexpr int sl_n = (14 + (o + 2) / 4) % 9, sl_p = (14 + (o + 1) / 4) % 9;
        if constexpr ((sl_n >> 2) != (sl_p >> 2)) setb(IC<(sl_n >> 2)>{});
      }
      if constexpr (o == PB_I) { if (w == 0) ald64c(fb, vfl + 256, rWS, zero); }
      asm volatile("s_waitcnt vmcnt(%1)" : "+v"(A_[o]) : "n"(PL.wAct[o]) : "memory");
      acc[0] = mfma(f[o % 3][0], A_[o], acc[0]);
      SCHED();
      if constexpr (o + 2 < 56) f[(o + 2) % 3][0] = rd(IC<o + 2>{}, 0);
      SCHED();
      acc[1] = mfma(f[o % 3][1], A_[o], acc[1]);
      SCHED();
      if constexpr (o + 2 < 56) f[(o + 2) % 3][1] = rd(IC<o + 2>{}, 1);
      if constexpr (o + AW < 56) a_load(IC<o + AW>{});
      SCHED();
      if constexpr ((o & 3) == 3 && pos <= 25) {
        if constexpr (o == PB_C) {
          if (w == 0) {
            asm volatile("s_waitcnt vmcnt(%1)" : "+v"(fb) : "n"(PL.wPollB) : "memory");
            if (!__all(fb.x == tlo && fb.y == thi)) wait_flags(rWS, vfl + 256, true, tlo, thi, 8);
          }
        }
        if constexpr (PL.wBar[pos] < 63) VMW(PL.wBar[pos]);
        SBAR();
        if constexpr (pos == 14) { if (tid == 0) set_flag(rWS, FL_A + (tt3 * 14 + fs3) * 2 + 1, tlo, thi); }
      }
    });
    {
      const f32x16 snd = kh ? acc[0] : acc[1];
      const u32 xb = L_XC4 + w * 4096 + lane * 16;
#pragma unroll
      for (int qq = 0; qq < 4; ++qq) { f32x4 v = {snd[4 * qq], snd[4 * qq + 1], snd[4 * qq + 2], snd[4 * qq + 3]}; L.wf4(xb + qq * 1024, v); }
    }
    RELANE();
    f32x4 gb4_[4];
    {
      const u32 tg = opq(L_TGB + (kh * 32 + 4 * h) * 4);
#pragma unroll
      for (int qq = 0; qq < 4; ++qq) gb4_[qq] = L.rf4(tg + 32 * qq);
    }
    BAR();
    {
      const f32x16 mine = kh ? acc[1] : acc[0];
      const u32 xb = L_XC4 + (w ^ 2) * 4096 + lane * 16;
      const u32 row = tt2 * 32 + t32, rsw = (row >> 1) & 7;
      const u32 ob = L_OUT + row * 128 + 8 * h, tg = opq(L_TGB + (kh * 32 + 4 * h) * 4);
      f32x4 o_[4];
#pragma unroll
      for (int qq = 0; qq < 4; ++qq) o_[qq] = L.rf4(xb + qq * 1024);
      sfor<0, 4>([&](auto qq) {
        constexpr int Q = qq.value;
        const f32x4 o = o_[Q];
        const f32x4 gb4 = gb4_[Q];
        v2u r = {pkv(fma2(lo2(gb4), pr<4 * Q>(mine) + lo2(o), unp(x1p[2 * Q]))), pkv(fma2(hi2(gb4), pr<4 * Q + 2>(mine) + hi2(o), unp(x1p[2 * Q + 1])))};
        L.w64(ob + (((kh * 4 + Q) ^ rsw) * 16), r);
      });
    }
    BAR();
    {
      const u32 rr = lane >> 3, c = lane & 7;
#pragma unroll
      for (int j = 0; j < 2; ++j) {
        const u32 row = w * 16 + 8 * j + rr;
        const v4u v = L.r128(L_OUT + row * 128 + c * 16);
        __builtin_amdgcn_raw_buffer_store_b128(v, rOUT, row * 896 + ((c ^ ((row >> 1) & 7)) * 16), (t64 * 64) * 896 + cs * 128, OUT_AUX);
      }
    }
    asm volatile(".Lfb_code1:");
  }
}

extern "C" void kernel_launch(void* const* d_in, const int* in_sizes, int n_in, void* d_out, int out_size, void* d_ws, size_t ws_size,
                              hipStream_t stream) {
  (void)in_sizes; (void)n_in; (void)out_size; (void)ws_size;
  (void)hipFuncSetAttribute((const void*)fused_block, hipFuncAttributeMaxDynamicSharedMemorySize, LDS_BYTES);
  fused_block<<<256, 512, LDS_BYTES, stream>>>((const u16*)d_in[0], (const u16*)d_in[1], (const u16*)d_in[2], (const u16*)d_in[3],
                                               (const u16*)d_in[5], (const u16*)d_in[6], (const u16*)d_in[7], (const u16*)d_in[8],
                                               (const u16*)d_in[9], (u16*)d_out, (unsigned char*)d_ws);
}
```
